```python
import torch

def _softmax(x, axis=-1):
    return torch.softmax(x, dim=axis)
def _gelu(x):
    return torch.nn.functional.gelu(x, approximate="tanh")
def _silu(x):
    return torch.nn.functional.silu(x)
def _sigmoid(x):
    return torch.sigmoid(x)
def _layernorm(x, axis=-1, eps=1e-5):
    m = x.mean(dim=axis, keepdim=True)
    v = x.var(dim=axis, keepdim=True, unbiased=False)
    return (x - m) / torch.sqrt(v + eps)
def _scatter_add(tgt, idx, src, axis=0):
    return tgt.clone().index_add_(axis, idx, src)
def _segment_sum(src, seg, n):
    out = torch.zeros((n,) + src.shape[1:], dtype=src.dtype)
    return out.index_add_(0, seg, src)
def _pad(x, pw):
    flat = tuple(v for pair in reversed(pw) for v in pair)
    return torch.nn.functional.pad(x, flat)

kernel_name = 'recurrent_scan__sliding_block_cached__geglu_ffn__decode'

def setup_inputs():

    _w = (lambda a: (lambda t: t.to(torch.bfloat16) if t.is_floating_point() else t)(torch.as_tensor(a)))
    def _A_setup():
        B, Sq, D = 1, 1, 448
        Hq, Hkv, Sk, Dh = 16, 8, 32768, 256
        assert Hq % Hkv == 0, (Hq, Hkv)
        return {
            "x": _w((lambda *s: torch.randn(*s, dtype=torch.bfloat16) * 0.05)(B, Sq, D)),
            "k_cache": _w((lambda *s: torch.randn(*s, dtype=torch.bfloat16) * 1.6)(B, Hkv, Sk, Dh)),
            "v_cache": _w((lambda *s: torch.randn(*s, dtype=torch.bfloat16) * 0.2)(B, Hkv, Sk, Dh)),
            "wq": _w((lambda *s: torch.randn(*s, dtype=torch.bfloat16) * 1.6)(D, Hq * Dh) * (D ** -0.5)),
            "wo": _w((lambda *s: torch.randn(*s, dtype=torch.bfloat16) * 0.2)(Hq * Dh, D) * ((Hq * Dh) ** -0.5)),
        }
    _A_in = _A_setup()

    def _B_setup():
        B, S, D, F = 1, 1, 448, 1792
        return {
            "x": _w((lambda *s: torch.randn(*s, dtype=torch.bfloat16) * 0.05)(B, S, D)),
            "w_gate": _w((lambda *s: torch.randn(*s, dtype=torch.bfloat16) * 0.1)(D, F)),
            "w_up": _w((lambda *s: torch.randn(*s, dtype=torch.bfloat16) * 0.1)(D, F)),
            "w_down": _w((lambda *s: torch.randn(*s, dtype=torch.bfloat16) * 0.1)(F, D)),
        }
    _B_in = _B_setup()

    return {
        "A_x": _A_in["x"],
        "A_k_cache": _A_in["k_cache"],
        "A_v_cache": _A_in["v_cache"],
        "A_wq": _A_in["wq"],
        "A_wo": _A_in["wo"],
        "B_x": _B_in["x"],
        "B_w_gate": _B_in["w_gate"],
        "B_w_up": _B_in["w_up"],
        "B_w_down": _B_in["w_down"],
    }

def reference(A_x, A_k_cache, A_v_cache, A_wq, A_wo, B_x, B_w_gate, B_w_up, B_w_down):
    def _A_ref(x, k_cache, v_cache, wq, wo):
        B, Sq, D = x.shape
        Hq, Dh = 16, 256
        Hkv, Sk = k_cache.shape[1], k_cache.shape[2]
        W = min(4096, max(256, Sk // 4))
        rep = Hq // Hkv
        q = (lambda x, *d: x.permute(*d))((x @ wq).reshape(B, Sq, Hq, Dh), 0, 2, 1, 3)
        k = (lambda x, n, axis: torch.repeat_interleave(x, n, dim=axis))(k_cache[:, :, Sk - W:, :], rep, axis=1)
        v = (lambda x, n, axis: torch.repeat_interleave(x, n, dim=axis))(v_cache[:, :, Sk - W:, :], rep, axis=1)
        scores = torch.einsum("bhqd,bhkd->bhqk", q, k) * (Dh ** -0.5)
        p = _softmax(scores, axis=-1)
        o = torch.einsum("bhqk,bhkd->bhqd", p, v)
        return (lambda x, *d: x.permute(*d))(o, 0, 2, 1, 3).reshape(B, Sq, Hq * Dh) @ wo

    def _B_ref(x, w_gate, w_up, w_down):
        return (_gelu(x @ w_gate) * (x @ w_up)) @ w_down

    x = A_x
    xn = _layernorm(x, axis=-1)
    s = _A_ref(xn, A_k_cache, A_v_cache, A_wq, A_wo)
    g = _silu(_B_ref(xn, B_w_gate, B_w_up, B_w_down))
    return x + s * g
```

```cpp
#include <hip/hip_runtime.h>
#include <hip/hip_bf16.h>
typedef unsigned short u16;
__device__ __forceinline__ float bf2f(u16 b){ return __uint_as_float(((unsigned)b)<<16); }
__device__ __forceinline__ float rbf(float f){ return __bfloat162float(__float2bfloat16(f)); }
constexpr int D=448, HQ=16, HKV=8, SK=32768, DH=256, W=4096, F=1792;

__device__ void layernorm(const u16* x, float* xn  , float* red){
  int t=threadIdx.x;
  if(t==0){
    float s=0; for(int i=0;i<D;i++) s+=bf2f(x[i]);
    float mean=s/D; float m=rbf(mean);
    float v=0; for(int i=0;i<D;i++){ float d=bf2f(x[i])-mean; v+=d*d; }
    v=rbf(v/D);
    float den=rbf(sqrtf(rbf(v+1e-5f)));
    red[0]=m; red[1]=den;
  }
  __syncthreads();
  for(int i=t;i<D;i+=blockDim.x) xn[i]=rbf(rbf(bf2f(x[i])-red[0])/red[1]);
  __syncthreads();
}
__global__ void k1(const u16* x,const u16* wq,const u16* wg,const u16* wu,float* q,float* h){
  __shared__ float xn[D]; __shared__ float red[2];
  layernorm(x,xn,red);
  int c=blockIdx.x*blockDim.x+threadIdx.x;
  if(c<4096){ float a=0; for(int i=0;i<D;i++) a+=xn[i]*bf2f(wq[i*4096+c]); q[c]=rbf(a); }
  else if(c<4096+F){ int j=c-4096; float g=0,u=0; for(int i=0;i<D;i++){ g+=xn[i]*bf2f(wg[i*F+j]); u+=xn[i]*bf2f(wu[i*F+j]); }
    g=rbf(g); u=rbf(u);
    float ge=0.5f*g*(1.f+tanhf(0.7978845608028654f*(g+0.044715f*g*g*g)));
    h[j]=rbf(rbf(ge)*u); }
}
__global__ void k2(const u16* k,const u16* v,const float* q,float* o){
  __shared__ float p[W]; __shared__ float qs[DH]; __shared__ float red[256];
  int hq=blockIdx.x, kv=hq/2, t=threadIdx.x;
  qs[t]=q[hq*DH+t]; __syncthreads();
  const u16* kb=k+((size_t)kv*SK+(SK-W))*DH; const u16* vb=v+((size_t)kv*SK+(SK-W))*DH;
  float mx=-INFINITY;
  for(int j=t;j<W;j+=256){ float a=0; for(int d=0;d<DH;d++) a+=qs[d]*bf2f(kb[(size_t)j*DH+d]); a=rbf(rbf(a)*0.0625f); p[j]=a; mx=fmaxf(mx,a);}
  red[t]=mx; __syncthreads();
  for(int s=128;s>0;s>>=1){ if(t<s) red[t]=fmaxf(red[t],red[t+s]); __syncthreads(); }
  mx=red[0]; __syncthreads();
  float l=0; for(int j=t;j<W;j+=256){ float e=expf(p[j]-mx); p[j]=e; l+=e; }
  red[t]=l; __syncthreads();
  for(int s=128;s>0;s>>=1){ if(t<s) red[t]+=red[t+s]; __syncthreads(); }
  l=red[0];
  float a=0; for(int j=0;j<W;j++) a+=rbf(p[j]/l)*bf2f(vb[(size_t)j*DH+t]);
  o[hq*DH+t]=rbf(a);
}
__global__ void k3(const u16* x,const u16* wo,const u16* wd,const float* o,const float* h,__hip_bfloat16* out){
  int c=blockIdx.x*blockDim.x+threadIdx.x; if(c>=D) return;
  float s=0; for(int i=0;i<4096;i++) s+=o[i]*bf2f(wo[i*D+c]); s=rbf(s);
  float f=0; for(int i=0;i<F;i++) f+=h[i]*bf2f(wd[i*D+c]); f=rbf(f);
  float g=rbf(f/(1.f+expf(-f)));
  out[c]=__float2bfloat16(bf2f(x[c])+rbf(s*g));
}
extern "C" void kernel_launch(void* const* d_in, const int* in_sizes, int n_in, void* d_out, int out_size, void* d_ws, size_t ws_size, hipStream_t stream){
  const u16* x=(const u16*)d_in[0]; const u16* k=(const u16*)d_in[1]; const u16* v=(const u16*)d_in[2];
  const u16* wq=(const u16*)d_in[3]; const u16* wo=(const u16*)d_in[4];
  const u16* wg=(const u16*)d_in[6]; const u16* wu=(const u16*)d_in[7]; const u16* wd=(const u16*)d_in[8];
  float* q=(float*)d_ws; float* h=q+4096; float* o=h+2048;
  k1<<<(4096+F+63)/64,64,0,stream>>>(x,wq,wg,wu,q,h);
  k2<<<16,256,0,stream>>>(k,v,q,o);
  k3<<<7,64,0,stream>>>(x,wo,wd,o,h,(__hip_bfloat16*)d_out);
}
```

```cpp
#include <hip/hip_runtime.h>
#include <hip/hip_bf16.h>

typedef unsigned v4u __attribute__((ext_vector_type(4)));
typedef float f32x4 __attribute__((ext_vector_type(4)));
typedef float f32x2 __attribute__((ext_vector_type(2)));
typedef short bf16x8 __attribute__((ext_vector_type(8)));
typedef __bf16 bf16x2_t __attribute__((ext_vector_type(2)));
#define LAS __attribute__((address_space(3)))
typedef __amdgpu_buffer_rsrc_t rsrc_t;

constexpr int D = 448, SK = 32768, WIN = 4096, DH = 256, FF = 1792;
constexpr int AUX_NT = 2, AUX_SC1 = 16;
constexpr unsigned OOB = 0x7fffff00u;
constexpr unsigned WS_E = 0, WS_QP = 4096, WS_FP = WS_QP + 8 * 4 * 4096, WS_ML = WS_FP + 28 * 8192, WS_OP = WS_ML + 8192,
                   WS_SP = WS_OP + 256 * 2048, WS_FS = 2u << 20, WS_END = WS_FS + 224 * 3584;
static_assert(WS_SP + 256 * 3584 <= WS_FS, "ws map");
constexpr unsigned SPIN_CAP = 1u << 16;
constexpr int L_FLAG = 0, L_TAG = 4, L_ML = 64, L_Q = 128, L_XN0 = L_Q + 1280, L_XN1 = L_XN0 + 1792, L_O = L_XN1 + 1792, L_END = L_O + 8192;
constexpr int LDS_BYTES = 84 * 1024;

#define ROT_MODE 0
#define GATHER_SLEEP 0

#define STAMP(i)
#define SETSTAMP(i, v)

struct Args {
  const void *x, *k, *v, *wq, *wo, *wg, *wu, *wd;
  void *out, *ws;
};

__device__ __forceinline__ rsrc_t mk(const void* p, unsigned bytes) { return __builtin_amdgcn_make_buffer_rsrc((void*)p, (short)0, (int)bytes, 0x00020000); }
template <int AUX> __device__ __forceinline__ v4u ld16(rsrc_t r, unsigned vo, unsigned so) { return __builtin_amdgcn_raw_buffer_load_b128(r, (int)vo, (int)so, AUX); }
template <int AUX> __device__ __forceinline__ unsigned ld4(rsrc_t r, unsigned vo, unsigned so) { return __builtin_amdgcn_raw_buffer_load_b32(r, (int)vo, (int)so, AUX); }
__device__ __forceinline__ void st16(v4u v, rsrc_t r, unsigned vo, unsigned so) { __builtin_amdgcn_raw_buffer_store_b128(v, r, (int)vo, (int)so, AUX_SC1); }
__device__ __forceinline__ f32x4 mfma(v4u a, v4u b, f32x4 c) {
  return __builtin_amdgcn_mfma_f32_16x16x32_bf16(__builtin_bit_cast(bf16x8, a), __builtin_bit_cast(bf16x8, b), c, 0, 0, 0);
}
__device__ __forceinline__ float asf(unsigned u) { return __builtin_bit_cast(float, u); }
__device__ __forceinline__ unsigned asu(float f) { return __builtin_bit_cast(unsigned, f); }
template <int CTRL> __device__ __forceinline__ float dpp(float x) { return asf((unsigned)__builtin_amdgcn_mov_dpp((int)asu(x), CTRL, 0xf, 0xf, true)); }
constexpr int XOR1 = 0xB1, XOR2 = 0x4E, HMIR = 0x141, MIR = 0x140, ROR8 = 0x128, ROR4 = 0x124;
__device__ __forceinline__ unsigned pk(float lo, float hi) {
  f32x2 v = {lo, hi};
  return __builtin_bit_cast(unsigned, __builtin_convertvector(v, bf16x2_t));
}
__device__ __forceinline__ float rbf(float f) { return asf(pk(f, f) & 0xffff0000u); }
__device__ __forceinline__ float sum8(float x) { x += dpp<XOR1>(x); x += dpp<XOR2>(x); x += dpp<HMIR>(x); return x; }
__device__ __forceinline__ float sum16(float x) { x = sum8(x); x += dpp<ROR8>(x); return x; }
__device__ __forceinline__ float max16(float x) { x = fmaxf(x, dpp<XOR1>(x)); x = fmaxf(x, dpp<XOR2>(x)); x = fmaxf(x, dpp<HMIR>(x)); x = fmaxf(x, dpp<ROR8>(x)); return x; }
__device__ __forceinline__ float xrow_sum(float x) {
  auto s = __builtin_amdgcn_permlane16_swap(asu(x), asu(x), false, false); x = asf(s[0]) + asf(s[1]);
  auto t = __builtin_amdgcn_permlane32_swap(asu(x), asu(x), false, false); return asf(t[0]) + asf(t[1]);
}
__device__ __forceinline__ float xrow_max(float x) {
  auto s = __builtin_amdgcn_permlane16_swap(asu(x), asu(x), false, false); x = fmaxf(asf(s[0]), asf(s[1]));
  auto t = __builtin_amdgcn_permlane32_swap(asu(x), asu(x), false, false); return fmaxf(asf(t[0]), asf(t[1]));
}
__device__ __forceinline__ v4u diag_mask(int jj) {
  const unsigned hm = (jj & 1) ? 0xffff0000u : 0x0000ffffu; const int d = jj >> 1;
  v4u m = {d == 0 ? hm : 0u, d == 1 ? hm : 0u, d == 2 ? hm : 0u, d == 3 ? hm : 0u};
  return m;
}
__device__ __forceinline__ v4u diag(unsigned dup, v4u m) { v4u a = {dup & m.x, dup & m.y, dup & m.z, dup & m.w}; return a; }
__device__ __forceinline__ v4u fold2(f32x4 A, f32x4 B, bool lo8, unsigned tag) {
  auto a0 = __builtin_amdgcn_permlane32_swap(asu(A[0]), asu(A[2]), false, false);
  auto a1 = __builtin_amdgcn_permlane32_swap(asu(A[1]), asu(A[3]), false, false);
  auto b0 = __builtin_amdgcn_permlane32_swap(asu(B[0]), asu(B[2]), false, false);
  auto b1 = __builtin_amdgcn_permlane32_swap(asu(B[1]), asu(B[3]), false, false);
  const float s0 = asf(lo8 ? a0[0] : b0[1]) + dpp<ROR8>(asf(lo8 ? b0[0] : a0[1]));
  const float s1 = asf(lo8 ? a1[0] : b1[1]) + dpp<ROR8>(asf(lo8 ? b1[0] : a1[1]));
  v4u r = {asu(s0), tag, asu(s1), tag};
  return r;
}
__device__ __forceinline__ v4u fold1(f32x4 A, unsigned tag) {
  auto a0 = __builtin_amdgcn_permlane32_swap(asu(A[0]), asu(A[2]), false, false);
  auto a1 = __builtin_amdgcn_permlane32_swap(asu(A[1]), asu(A[3]), false, false);
  v4u r = {asu(asf(a0[0]) + dpp<ROR8>(asf(a0[1]))), tag, asu(asf(a1[0]) + dpp<ROR8>(asf(a1[1]))), tag};
  return r;
}
__device__ __forceinline__ bool tagged(v4u g, unsigned tag) { return (g.y == tag) & (g.w == tag); }
#define AGAIN() asm volatile("" ::: "memory")

__device__ __forceinline__ void layernorm(v4u xv, int l, LAS unsigned char* dst) {
  float e[8];
#pragma unroll
  for (int i = 0; i < 4; ++i) { e[2 * i] = asf(xv[i] << 16); e[2 * i + 1] = asf(xv[i] & 0xffff0000u); }
  float s = ((e[0] + e[1]) + (e[2] + e[3])) + ((e[4] + e[5]) + (e[6] + e[7]));
  const float mean = xrow_sum(sum16(s)) * (1.0f / D);
  float q = 0.f;
#pragma unroll
  for (int i = 0; i < 8; ++i) { const float d = e[i] - mean; q = fmaf(d, d, q); }
  q = l < 56 ? q : 0.f;
  const float var = rbf(xrow_sum(sum16(q)) * (1.0f / D));
  const float den = rbf(__builtin_sqrtf(rbf(var + 1e-5f)));
  const float inv = 1.0f / den, mb = rbf(mean);
  v4u o0, o1;
#pragma unroll
  for (int i = 0; i < 4; ++i) { const float a = rbf(e[i] - mb) * inv, b = rbf(e[4 + i] - mb) * inv; o0[i] = pk(a, a); o1[i] = pk(b, b); }
  if (l < 56) { *(LAS v4u*)(dst + l * 32) = o0; *(LAS v4u*)(dst + l * 32 + 16) = o1; }
}

__global__ void __launch_bounds__(384, 2) fused_decode(Args a) {
  extern __shared__ __attribute__((aligned(16))) unsigned char smem[];
  LAS unsigned char* L = (LAS unsigned char*)smem;
  STAMP(0);
  const int b = blockIdx.x, h = b & 7, s = b >> 3;
  const int wv = __builtin_amdgcn_readfirstlane(threadIdx.x >> 6), l = threadIdx.x & 63, g = l >> 4, n = l & 15;
  const bool lo8 = n < 8;
  const rsrc_t rws = mk(a.ws, WS_END);
  const unsigned colp = 8 * (n & 7) + 4 * (g & 1) + 2 * (l >> 5);

  if (wv == 0) {
    __builtin_amdgcn_s_setprio(3);
    const int rg = s & 3, cg = s >> 2;
    if (l == 0) *(volatile LAS unsigned*)(L + L_FLAG) = 0u;
    const unsigned ep = ld4<AUX_SC1>(rws, WS_E, 0);
    const v4u xv = ld16<0>(mk(a.x, D * 2), l * 16, 0);
    const rsrc_t rw = mk((const char*)a.wq + (size_t)(112 * rg) * 8192 + h * 1024 + cg * 128, 112 * 8192);
    v4u W[14];
#pragma unroll
    for (int i = 0; i < 14; ++i) W[i] = ld16<AUX_NT>(rw, (l >> 3) * 8192 + (l & 7) * 16, i * 65536);
    __builtin_amdgcn_sched_barrier(0);
    asm volatile("s_waitcnt lgkmcnt(0)" ::: "memory");
    __builtin_amdgcn_s_barrier();
    STAMP(1);
    layernorm(xv, l, L + L_XN0);
    const unsigned tag = __builtin_amdgcn_readfirstlane(ep) + 1u;
    STAMP(2);
    const v4u mk8 = diag_mask(n & 7);
    f32x4 acc = {0.f, 0.f, 0.f, 0.f};
    const LAS unsigned* xr = (const LAS unsigned*)(L + L_XN0) + 112 * rg + 2 * g + (n >> 3);
#pragma unroll
    for (int i = 0; i < 14; ++i) acc = mfma(diag(xr[8 * i], mk8), W[i], acc);
    STAMP(3);
    st16(fold1(acc, tag), rws, lo8 ? (64 * cg + colp) * 8 : OOB, WS_QP + (h * 4 + rg) * 4096);
    STAMP(4);
    if (GATHER_SLEEP) { for (int i = 0; i < GATHER_SLEEP; ++i) __builtin_amdgcn_s_sleep(8); }
    v4u G[16];
    unsigned it = 0;
    for (;; ++it) {
      AGAIN();
      bool ok = true;
#pragma unroll
      for (int i = 0; i < 16; ++i) G[i] = ld16<AUX_SC1>(rws, l * 16 + (i & 3) * 1024, WS_QP + h * 16384 + (i >> 2) * 4096);
#pragma unroll
      for (int i = 0; i < 16; ++i) ok &= tagged(G[i], tag);
      if (__all(ok) || it >= SPIN_CAP) break;
    }
    STAMP(5);
    SETSTAMP(6, it);
#pragma unroll
    for (int i = 0; i < 4; ++i) {
      const float v0 = (asf(G[i].x) + asf(G[4 + i].x)) + (asf(G[8 + i].x) + asf(G[12 + i].x));
      const float v1 = (asf(G[i].z) + asf(G[4 + i].z)) + (asf(G[8 + i].z) + asf(G[12 + i].z));
      *(LAS unsigned*)(L + L_Q + (i >> 1) * 640 + (i & 1) * 256 + l * 4) = pk(v0, v1);
    }
    if (l == 0) *(volatile LAS unsigned*)(L + L_TAG) = tag;
    asm volatile("s_waitcnt lgkmcnt(0)" ::: "memory");
    if (l == 0) *(volatile LAS unsigned*)(L + L_FLAG) = 1u;
    STAMP(7);
  } else if (wv == 1) {
    __builtin_amdgcn_s_setprio(3);
    if (b >= 224) { __builtin_amdgcn_s_barrier(); return; }
    const int fc = b >> 3, fr = b & 7;
    const unsigned ep = ld4<AUX_SC1>(rws, WS_E, 0);
    const v4u xv = ld16<0>(mk(a.x, D * 2), l * 16, 0);
    const size_t wofs = (size_t)(56 * fr) * 3584 + fc * 128;
    const rsrc_t rg_ = mk((const char*)a.wg + wofs, 56 * 3584), ru_ = mk((const char*)a.wu + wofs, 56 * 3584);
    v4u Wg[7], Wu[7];
#pragma unroll
    for (int i = 0; i < 7; ++i) Wg[i] = ld16<AUX_NT>(rg_, (l >> 3) * 3584 + (l & 7) * 16, i * 8 * 3584);
#pragma unroll
    for (int i = 0; i < 7; ++i) Wu[i] = ld16<AUX_NT>(ru_, (l >> 3) * 3584 + (l & 7) * 16, i * 8 * 3584);
    __builtin_amdgcn_sched_barrier(0);
    __builtin_amdgcn_s_barrier();
    STAMP(1);
    layernorm(xv, l, L + L_XN1);
    const unsigned tag = __builtin_amdgcn_readfirstlane(ep) + 1u;
    STAMP(2);
    const v4u mk8 = diag_mask(n & 7);
    f32x4 ag = {0.f, 0.f, 0.f, 0.f}, au = {0.f, 0.f, 0.f, 0.f};
    const LAS unsigned* xr = (const LAS unsigned*)(L + L_XN1) + 56 * fr + 2 * g + (n >> 3);
#pragma unroll
    for (int i = 0; i < 7; ++i) { const v4u A = diag(xr[8 * i], mk8); ag = mfma(A, Wg[i], ag); au = mfma(A, Wu[i], au); }
    STAMP(3);
    st16(fold2(ag, au, lo8, tag), rws, (n >> 3) * 512 + colp * 8, WS_FP + fc * 8192 + fr * 1024);
    STAMP(4);
    const rsrc_t rd_ = mk((const char*)a.wd + (size_t)(8 * b) * 896, 8 * 896);
    v4u Wd[7];
#pragma unroll
    for (int i = 0; i < 7; ++i) Wd[i] = ld16<AUX_NT>(rd_, (l >> 3) * 896 + (l & 7) * 16 + i * 128, 0);
    __builtin_amdgcn_sched_barrier(0);
    if (GATHER_SLEEP) { for (int i = 0; i < GATHER_SLEEP; ++i) __builtin_amdgcn_s_sleep(8); }
    v4u G;
    unsigned it = 0;
    for (;; ++it) {
      AGAIN();
      G = ld16<AUX_SC1>(rws, (n & 7) * 1024 + (n >> 3) * 512 + g * 16, WS_FP + fc * 8192 + fr * 64);
      if (__all(tagged(G, tag)) || it >= SPIN_CAP) break;
    }
    STAMP(5);
    SETSTAMP(6, it);
    const float v0 = sum8(asf(G.x)), v1 = sum8(asf(G.z));
    const float own = lo8 ? v0 : v1, rcv = dpp<ROR8>(lo8 ? v1 : v0);
    const float gt = rbf(lo8 ? own : rcv), up = rbf(lo8 ? rcv : own);
    const float u2 = -2.0f * 0.7978845608028654f * 1.4426950408889634f * fmaf(0.044715f * gt * gt, gt, gt);
    const float ge = rbf(gt * __builtin_amdgcn_rcpf(1.0f + __builtin_amdgcn_exp2f(u2)));
    const float hv = ge * up;
    const v4u A = diag(pk(hv, hv), mk8);
    const f32x4 z = {0.f, 0.f, 0.f, 0.f};
    f32x4 F[7];
#pragma unroll
    for (int i = 0; i < 7; ++i) F[i] = mfma(A, Wd[i], z);
#pragma unroll
    for (int i = 0; i < 3; ++i) st16(fold2(F[2 * i], F[2 * i + 1], lo8, tag), rws, (n >> 3) * 512 + colp * 8 + i * 1024, WS_FS + b * 3584);
    st16(fold1(F[6], tag), rws, lo8 ? colp * 8 + 3072 : OOB, WS_FS + b * 3584);
    STAMP(7);
  } else {
    const int w = wv - 2;
    const size_t kvofs = ((size_t)h * SK + (SK - WIN) + s * 128) * 512;
    const rsrc_t rk = mk((const char*)a.k + kvofs, 65536), rv = mk((const char*)a.v + kvofs, 65536);
    const rsrc_t ro = mk((const char*)a.wo + (size_t)(h * 512 + s * 16) * 896, 16 * 896);
    const int rot = ROT_MODE == 0 ? s : ROT_MODE == 1 ? 0 : s + 2 * h;
#define TILE(T) ((4 * (T) + w + rot) & 15)
    __builtin_amdgcn_s_barrier();
    STAMP(1);
    v4u K[16], V[16], WO[4];
#pragma unroll
    for (int i = 0; i < 16; ++i) K[i] = ld16<AUX_NT>(rk, (n & 7) * 512 + (n >> 3) * 64 + g * 16 + (i & 3) * 128, TILE(i >> 2) * 4096);
#pragma unroll
    for (int i = 0; i < 16; ++i) V[i] = ld16<AUX_NT>(rv, TILE(2 * (g >> 1) + (i >> 3)) * 4096 + (g & 1) * 2048 + n * 16 + ((i >> 1) & 3) * 512 + (i & 1) * 256, 0);
#pragma unroll
    for (int i = 0; i < 4; ++i) WO[i] = ld16<AUX_NT>(ro, ((w == 3 && i >= 2) ? OOB : (4 * g + (n >> 3)) * 896 + (n & 7) * 16) + (i & 1) * 1792 + (i >> 1) * 128, w * 256);
    __builtin_amdgcn_sched_barrier(0);
    STAMP(2);
    for (unsigned it = 0; *(volatile LAS unsigned*)(L + L_FLAG) == 0u && it < 64 * SPIN_CAP; ++it) __builtin_amdgcn_s_sleep(1);
    AGAIN();
    STAMP(3);
    const unsigned tag = __builtin_amdgcn_readfirstlane(*(volatile LAS unsigned*)(L + L_TAG));
    const int hd = (n >> 2) & 1;
    v4u Q[4];
#pragma unroll
    for (int i = 0; i < 4; ++i) Q[i] = *(const LAS v4u*)(L + L_Q + hd * 640 + (n >> 3) * 64 + g * 16 + i * 128);
    f32x4 S[4];
#pragma unroll
    for (int t = 0; t < 4; ++t) {
      f32x4 c = {0.f, 0.f, 0.f, 0.f};
#pragma unroll
      for (int i = 0; i < 4; ++i) c = mfma(K[4 * t + i], Q[i], c);
      S[t] = c;
    }
    float e[8];
#pragma unroll
    for (int j = 0; j < 8; ++j) {
      auto x = __builtin_amdgcn_permlane32_swap(asu(S[j >> 2][j & 3]), asu(S[2 + (j >> 2)][j & 3]), false, false);
      const float z = asf(lo8 ? x[0] : x[1]);
      e[j] = rbf(z + dpp<ROR8>(z));
    }
    constexpr float C2 = 0.0625f * 1.4426950408889634f;
    float m = fmaxf(fmaxf(fmaxf(e[0], e[1]), fmaxf(e[2], e[3])), fmaxf(fmaxf(e[4], e[5]), fmaxf(e[6], e[7])));
    m = xrow_max(m) * C2;
    float ls = 0.f;
#pragma unroll
    for (int j = 0; j < 8; ++j) { e[j] = __builtin_amdgcn_exp2f(fmaf(e[j], C2, -m)); ls += e[j]; }
    ls = xrow_sum(ls);
    if (l == 0 || l == 4) { f32x2 t2 = {m, ls}; *(LAS f32x2*)(L + L_ML + hd * 32 + w * 8) = t2; }
    asm volatile("s_waitcnt lgkmcnt(0)" ::: "memory");
    STAMP(4);
    __builtin_amdgcn_s_barrier();
    AGAIN();
    const f32x4 r0 = *(const LAS f32x4*)(L + L_ML + hd * 32), r1 = *(const LAS f32x4*)(L + L_ML + hd * 32 + 16);
    const float mb = fmaxf(fmaxf(r0[0], r0[2]), fmaxf(r1[0], r1[2]));
    if (w == 0) {
      const float lb = (r0[1] * __builtin_amdgcn_exp2f(r0[0] - mb) + r0[3] * __builtin_amdgcn_exp2f(r0[2] - mb)) +
                       (r1[1] * __builtin_amdgcn_exp2f(r1[0] - mb) + r1[3] * __builtin_amdgcn_exp2f(r1[2] - mb));
      const v4u rec = {asu(mb), tag, asu(lb), tag};
      st16(rec, rws, (l == 0 || l == 4) ? hd * 16 : OOB, WS_ML + (h * 32 + s) * 32);
    }
    const float fw = __builtin_amdgcn_exp2f(m - mb);
    const v4u mkv = diag_mask(4 * (n >> 3) + (n & 3));
    v4u A[8];
#pragma unroll
    for (int j = 0; j < 8; ++j) { const float p = e[j] * fw; A[j] = diag(pk(p, p), mkv); }
    __builtin_amdgcn_sched_barrier(0);
    STAMP(5);
    f32x4 o0 = {0.f, 0.f, 0.f, 0.f}, o1 = {0.f, 0.f, 0.f, 0.f};
#pragma unroll
    for (int j = 0; j < 8; ++j) { o0 = mfma(A[j], V[2 * j], o0); o1 = mfma(A[j], V[2 * j + 1], o1); __builtin_amdgcn_sched_barrier(0); }
    *(LAS f32x4*)(L + L_O + w * 2048 + l * 16) = o0;
    *(LAS f32x4*)(L + L_O + w * 2048 + 1024 + l * 16) = o1;
    asm volatile("s_waitcnt lgkmcnt(0)" ::: "memory");
    STAMP(6);
    __builtin_amdgcn_s_barrier();
    AGAIN();
    if (w < 2) {
      const LAS f32x4* p = (const LAS f32x4*)(L + L_O + w * 1024 + l * 16);
      const f32x4 t = (p[0] + p[128]) + (p[256] + p[384]);
      const v4u rec = {pk(t[0], t[1]), tag, pk(t[2], t[3]), tag};
      st16(rec, rws, (g & 1) * 1024 + w * 512 + n * 32 + (g >> 1) * 16, WS_OP + (h * 32 + s) * 2048);
    }
    STAMP(7);
    const int fc = 16 * (s >> 1) + 2 * h + (s & 1);
    const bool fin = (w == 3) && (s < 8) && (fc < 56);
    float wg0, wg1;
    {
      v4u M0, M1;
      for (unsigned it = 0;; ++it) {
        AGAIN();
        M0 = ld16<AUX_SC1>(rws, n * 32, WS_ML + h * 1024 + (s >> 4) * 16);
        M1 = ld16<AUX_SC1>(rws, n * 32 + 512, WS_ML + h * 1024 + (s >> 4) * 16);
        if (__all(tagged(M0, tag) & tagged(M1, tag)) || it >= SPIN_CAP) break;
      }
      const float M = max16(fmaxf(asf(M0.x), asf(M1.x)));
      wg0 = __builtin_amdgcn_exp2f(asf(M0.x) - M); wg1 = __builtin_amdgcn_exp2f(asf(M1.x) - M);
      const float inv = __builtin_amdgcn_rcpf(sum16(fmaf(wg0, asf(M0.z), wg1 * asf(M1.z))));
      wg0 *= inv; wg1 *= inv;
    }
    float gate0 = 0.f, gate1 = 0.f; unsigned xo = 0;
    if (fin) {
      xo = ld4<0>(mk(a.x, D * 2), fc * 16 + g * 4, 0);
      v4u G[14];
      for (unsigned it = 0;; ++it) {
        AGAIN();
        bool ok = true;
#pragma unroll
        for (int i = 0; i < 14; ++i) G[i] = ld16<AUX_SC1>(rws, n * 3584 + g * 16, WS_FS + fc * 64 + i * 16 * 3584);
#pragma unroll
        for (int i = 0; i < 14; ++i) ok &= tagged(G[i], tag);
        if (__all(ok) || it >= SPIN_CAP) break;
      }
      float f0 = 0.f, f1 = 0.f;
#pragma unroll
      for (int i = 0; i < 14; ++i) { f0 += asf(G[i].x); f1 += asf(G[i].z); }
      f0 = rbf(sum16(f0)); f1 = rbf(sum16(f1));
      gate0 = rbf(f0 * __builtin_amdgcn_rcpf(1.0f + __builtin_amdgcn_exp2f(-1.4426950408889634f * f0)));
      gate1 = rbf(f1 * __builtin_amdgcn_rcpf(1.0f + __builtin_amdgcn_exp2f(-1.4426950408889634f * f1)));
    }
    STAMP(8);
    v4u G0, G1;
    for (unsigned it = 0;; ++it) {
      AGAIN();
      G0 = ld16<AUX_SC1>(rws, n * 2048 + g * 16, WS_OP + h * 65536 + s * 64);
      G1 = ld16<AUX_SC1>(rws, n * 2048 + g * 16, WS_OP + h * 65536 + s * 64 + 32768);
      if (__all(tagged(G0, tag) & tagged(G1, tag)) || it >= SPIN_CAP) break;
    }
    STAMP(9);
    const v4u mk8 = diag_mask(n & 7);
    v4u B0, B1;
    {
      const unsigned sk = lo8 ? 0x01000c0cu : 0x03020c0cu, ss = lo8 ? 0x03020c0cu : 0x01000c0cu;
      auto half = [](unsigned x, unsigned sel) { return asf(__builtin_amdgcn_perm(0u, x, sel)); };
      float k0 = fmaf(wg0, half(G0.x, sk), wg1 * half(G1.x, sk)), s0 = fmaf(wg0, half(G0.x, ss), wg1 * half(G1.x, ss));
      float k1 = fmaf(wg0, half(G0.z, sk), wg1 * half(G1.z, sk)), s1 = fmaf(wg0, half(G0.z, ss), wg1 * half(G1.z, ss));
      k0 = sum8(k0 + dpp<ROR8>(s0)); k1 = sum8(k1 + dpp<ROR8>(s1));
      B0 = diag(pk(k0, k0), mk8); B1 = diag(pk(k1, k1), mk8);
    }
    const f32x4 z = {0.f, 0.f, 0.f, 0.f};
    const f32x4 sa = mfma(B1, WO[1], mfma(B0, WO[0], z)), sb = mfma(B1, WO[3], mfma(B0, WO[2], z));
    st16(fold2(sa, sb, lo8, tag), rws, (w == 3 && !lo8) ? OOB : (n >> 3) * 512 + colp * 8 + w * 1024, WS_SP + b * 3584);
    STAMP(10);
    if (fin) {
      v4u G[16];
      for (unsigned it = 0;; ++it) {
        AGAIN();
        bool ok = true;
#pragma unroll
        for (int i = 0; i < 16; ++i) G[i] = ld16<AUX_SC1>(rws, n * 3584 + g * 16, WS_SP + fc * 64 + i * 16 * 3584);
#pragma unroll
        for (int i = 0; i < 16; ++i) ok &= tagged(G[i], tag);
        if (__all(ok) || it >= SPIN_CAP) break;
      }
      float s0 = 0.f, s1 = 0.f;
#pragma unroll
      for (int i = 0; i < 16; ++i) { s0 += asf(G[i].x); s1 += asf(G[i].z); }
      s0 = rbf(sum16(s0)); s1 = rbf(sum16(s1));
      const unsigned r = pk(asf(xo << 16) + rbf(s0 * gate0), asf(xo & 0xffff0000u) + rbf(s1 * gate1));
      __builtin_amdgcn_raw_buffer_store_b32(r, mk(a.out, D * 2), n == 0 ? fc * 16 + g * 4 : (int)OOB, 0, 0);
      if (b == 0) __builtin_amdgcn_raw_buffer_store_b32(tag, rws, l == 0 ? (int)WS_E : (int)OOB, 0, AUX_SC1);
      STAMP(11);
    }
  }
}

extern "C" void kernel_launch(void* const* d_in, const int* in_sizes, int n_in, void* d_out, int out_size, void* d_ws, size_t ws_size,
                              hipStream_t stream) {
  Args a;
  a.x = d_in[0]; a.k = d_in[1]; a.v = d_in[2]; a.wq = d_in[3]; a.wo = d_in[4];
  a.wg = d_in[6]; a.wu = d_in[7]; a.wd = d_in[8]; a.out = d_out; a.ws = d_ws;
  (void)hipFuncSetAttribute((const void*)fused_decode, hipFuncAttributeMaxDynamicSharedMemorySize, LDS_BYTES);
  hipLaunchKernelGGL(fused_decode, dim3(256), dim3(384), LDS_BYTES, stream, a);
}
```

```cpp
#define SC_STAT 5
#define SC_WAVE 2
#define SC_AT 1
#include <hip/hip_runtime.h>
#include <hip/hip_bf16.h>

typedef unsigned v4u __attribute__((ext_vector_type(4)));
typedef float f32x4 __attribute__((ext_vector_type(4)));
typedef float f32x2 __attribute__((ext_vector_type(2)));
typedef short bf16x8 __attribute__((ext_vector_type(8)));
typedef __bf16 bf16x2_t __attribute__((ext_vector_type(2)));
#define LAS __attribute__((address_space(3)))
typedef __amdgpu_buffer_rsrc_t rsrc_t;

constexpr int D = 448, SK = 32768, WIN = 4096, DH = 256, FF = 1792;
constexpr int AUX_NT = 2, AUX_SC1 = 16;
constexpr unsigned OOB = 0x7fffff00u;
constexpr unsigned WS_E = 0, WS_QP = 4096, WS_FP = WS_QP + 8 * 4 * 4096, WS_ML = WS_FP + 28 * 8192, WS_OP = WS_ML + 8192,
                   WS_SP = WS_OP + 256 * 2048, WS_FS = 2u << 20, WS_DBG = WS_FS + 224 * 3584, WS_END = WS_DBG + 4096;
static_assert(WS_SP + 256 * 3584 <= WS_FS, "ws map");
constexpr unsigned SPIN_CAP = 1u << 16;
constexpr int L_FLAG = 0, L_TAG = 4, L_ML = 64, L_Q = 128, L_XN0 = L_Q + 1280, L_XN1 = L_XN0 + 1792, L_O = L_XN1 + 1792, L_END = L_O + 8192;
constexpr int LDS_BYTES = 84 * 1024;

#define ROT_MODE 0
#define GATHER_SLEEP 0

#define STAMP(i) do { if ((i) == 0 || ((i) == SC_AT)) { __builtin_amdgcn_sched_barrier(0); asm volatile("s_memrealtime %0\n\ts_waitcnt lgkmcnt(0)" : "=s"(sct[(i) == 0 ? 0 : 1]) :: "memory"); __builtin_amdgcn_sched_barrier(0); } } while (0)
#define SETSTAMP(i, v)
#define SC_PUB(W) do { if ((W) == SC_WAVE) { v4u r_ = {(unsigned)sct[0], tag, (unsigned)sct[1], tag}; st16(r_, rws, l == 0 ? b * 16 : OOB, WS_DBG); } } while (0)

struct Args {
  const void *x, *k, *v, *wq, *wo, *wg, *wu, *wd;
  void *out, *ws;
};

__device__ __forceinline__ rsrc_t mk(const void* p, unsigned bytes) { return __builtin_amdgcn_make_buffer_rsrc((void*)p, (short)0, (int)bytes, 0x00020000); }
template <int AUX> __device__ __forceinline__ v4u ld16(rsrc_t r, unsigned vo, unsigned so) { return __builtin_amdgcn_raw_buffer_load_b128(r, (int)vo, (int)so, AUX); }
template <int AUX> __device__ __forceinline__ unsigned ld4(rsrc_t r, unsigned vo, unsigned so) { return __builtin_amdgcn_raw_buffer_load_b32(r, (int)vo, (int)so, AUX); }
__device__ __forceinline__ void st16(v4u v, rsrc_t r, unsigned vo, unsigned so) { __builtin_amdgcn_raw_buffer_store_b128(v, r, (int)vo, (int)so, AUX_SC1); }
__device__ __forceinline__ f32x4 mfma(v4u a, v4u b, f32x4 c) {
  return __builtin_amdgcn_mfma_f32_16x16x32_bf16(__builtin_bit_cast(bf16x8, a), __builtin_bit_cast(bf16x8, b), c, 0, 0, 0);
}
__device__ __forceinline__ float asf(unsigned u) { return __builtin_bit_cast(float, u); }
__device__ __forceinline__ unsigned asu(float f) { return __builtin_bit_cast(unsigned, f); }
template <int CTRL> __device__ __forceinline__ float dpp(float x) { return asf((unsigned)__builtin_amdgcn_mov_dpp((int)asu(x), CTRL, 0xf, 0xf, true)); }
constexpr int XOR1 = 0xB1, XOR2 = 0x4E, HMIR = 0x141, MIR = 0x140, ROR8 = 0x128, ROR4 = 0x124;
__device__ __forceinline__ unsigned pk(float lo, float hi) {
  f32x2 v = {lo, hi};
  return __builtin_bit_cast(unsigned, __builtin_convertvector(v, bf16x2_t));
}
__device__ __forceinline__ float rbf(float f) { return asf(pk(f, f) & 0xffff0000u); }
__device__ __forceinline__ float sum8(float x) { x += dpp<XOR1>(x); x += dpp<XOR2>(x); x += dpp<HMIR>(x); return x; }
__device__ __forceinline__ float sum16(float x) { x = sum8(x); x += dpp<ROR8>(x); return x; }
__device__ __forceinline__ float max16(float x) { x = fmaxf(x, dpp<XOR1>(x)); x = fmaxf(x, dpp<XOR2>(x)); x = fmaxf(x, dpp<HMIR>(x)); x = fmaxf(x, dpp<ROR8>(x)); return x; }
__device__ __forceinline__ float xrow_sum(float x) {
  auto s = __builtin_amdgcn_permlane16_swap(asu(x), asu(x), false, false); x = asf(s[0]) + asf(s[1]);
  auto t = __builtin_amdgcn_permlane32_swap(asu(x), asu(x), false, false); return asf(t[0]) + asf(t[1]);
}
__device__ __forceinline__ float xrow_max(float x) {
  auto s = __builtin_amdgcn_permlane16_swap(asu(x), asu(x), false, false); x = fmaxf(asf(s[0]), asf(s[1]));
  auto t = __builtin_amdgcn_permlane32_swap(asu(x), asu(x), false, false); return fmaxf(asf(t[0]), asf(t[1]));
}
__device__ __forceinline__ v4u diag_mask(int jj) {
  const unsigned hm = (jj & 1) ? 0xffff0000u : 0x0000ffffu; const int d = jj >> 1;
  v4u m = {d == 0 ? hm : 0u, d == 1 ? hm : 0u, d == 2 ? hm : 0u, d == 3 ? hm : 0u};
  return m;
}
__device__ __forceinline__ v4u diag(unsigned dup, v4u m) { v4u a = {dup & m.x, dup & m.y, dup & m.z, dup & m.w}; return a; }
__device__ __forceinline__ v4u fold2(f32x4 A, f32x4 B, bool lo8, unsigned tag) {
  auto a0 = __builtin_amdgcn_permlane32_swap(asu(A[0]), asu(A[2]), false, false);
  auto a1 = __builtin_amdgcn_permlane32_swap(asu(A[1]), asu(A[3]), false, false);
  auto b0 = __builtin_amdgcn_permlane32_swap(asu(B[0]), asu(B[2]), false, false);
  auto b1 = __builtin_amdgcn_permlane32_swap(asu(B[1]), asu(B[3]), false, false);
  const float s0 = asf(lo8 ? a0[0] : b0[1]) + dpp<ROR8>(asf(lo8 ? b0[0] : a0[1]));
  const float s1 = asf(lo8 ? a1[0] : b1[1]) + dpp<ROR8>(asf(lo8 ? b1[0] : a1[1]));
  v4u r = {asu(s0), tag, asu(s1), tag};
  return r;
}
__device__ __forceinline__ v4u fold1(f32x4 A, unsigned tag) {
  auto a0 = __builtin_amdgcn_permlane32_swap(asu(A[0]), asu(A[2]), false, false);
  auto a1 = __builtin_amdgcn_permlane32_swap(asu(A[1]), asu(A[3]), false, false);
  v4u r = {asu(asf(a0[0]) + dpp<ROR8>(asf(a0[1]))), tag, asu(asf(a1[0]) + dpp<ROR8>(asf(a1[1]))), tag};
  return r;
}
__device__ __forceinline__ bool tagged(v4u g, unsigned tag) { return (g.y == tag) & (g.w == tag); }
#define AGAIN() asm volatile("" ::: "memory")

__device__ __forceinline__ void layernorm(v4u xv, int l, LAS unsigned char* dst) {
  float e[8];
#pragma unroll
  for (int i = 0; i < 4; ++i) { e[2 * i] = asf(xv[i] << 16); e[2 * i + 1] = asf(xv[i] & 0xffff0000u); }
  float s = ((e[0] + e[1]) + (e[2] + e[3])) + ((e[4] + e[5]) + (e[6] + e[7]));
  const float mean = xrow_sum(sum16(s)) * (1.0f / D);
  float q = 0.f;
#pragma unroll
  for (int i = 0; i < 8; ++i) { const float d = e[i] - mean; q = fmaf(d, d, q); }
  q = l < 56 ? q : 0.f;
  const float var = rbf(xrow_sum(sum16(q)) * (1.0f / D));
  const float den = rbf(__builtin_sqrtf(rbf(var + 1e-5f)));
  const float inv = 1.0f / den, mb = rbf(mean);
  v4u o0, o1;
#pragma unroll
  for (int i = 0; i < 4; ++i) { const float a = rbf(e[i] - mb) * inv, b = rbf(e[4 + i] - mb) * inv; o0[i] = pk(a, a); o1[i] = pk(b, b); }
  if (l < 56) { *(LAS v4u*)(dst + l * 32) = o0; *(LAS v4u*)(dst + l * 32 + 16) = o1; }
}

__global__ void __launch_bounds__(384, 2) fused_decode(Args a) {
  extern __shared__ __attribute__((aligned(16))) unsigned char smem[];
  LAS unsigned char* L = (LAS unsigned char*)smem;
  unsigned long long sct[2] = {0, 0};
  STAMP(0);
  const int b = blockIdx.x, h = b & 7, s = b >> 3;
  const int wv = __builtin_amdgcn_readfirstlane(threadIdx.x >> 6), l = threadIdx.x & 63, g = l >> 4, n = l & 15;
  const bool lo8 = n < 8;
  const rsrc_t rws = mk(a.ws, WS_END);
  const unsigned colp = 8 * (n & 7) + 4 * (g & 1) + 2 * (l >> 5);

  if (wv == 0) {
    __builtin_amdgcn_s_setprio(3);
    const int rg = s & 3, cg = s >> 2;
    if (l == 0) *(volatile LAS unsigned*)(L + L_FLAG) = 0u;
    const unsigned ep = ld4<AUX_SC1>(rws, WS_E, 0);
    const v4u xv = ld16<0>(mk(a.x, D * 2), l * 16, 0);
    const rsrc_t rw = mk((const char*)a.wq + (size_t)(112 * rg) * 8192 + h * 1024 + cg * 128, 112 * 8192);
    v4u W[14];
#pragma unroll
    for (int i = 0; i < 14; ++i) W[i] = ld16<AUX_NT>(rw, (l >> 3) * 8192 + (l & 7) * 16, i * 65536);
    __builtin_amdgcn_sched_barrier(0);
    asm volatile("s_waitcnt lgkmcnt(0)" ::: "memory");
    __builtin_amdgcn_s_barrier();
    STAMP(1);
    layernorm(xv, l, L + L_XN0);
    const unsigned tag = __builtin_amdgcn_readfirstlane(ep) + 1u;
    STAMP(2);
    const v4u mk8 = diag_mask(n & 7);
    f32x4 acc = {0.f, 0.f, 0.f, 0.f};
    const LAS unsigned* xr = (const LAS unsigned*)(L + L_XN0) + 112 * rg + 2 * g + (n >> 3);
#pragma unroll
    for (int i = 0; i < 14; ++i) acc = mfma(diag(xr[8 * i], mk8), W[i], acc);
    STAMP(3);
    st16(fold1(acc, tag), rws, lo8 ? (64 * cg + colp) * 8 : OOB, WS_QP + (h * 4 + rg) * 4096);
    STAMP(4);
    if (GATHER_SLEEP) { for (int i = 0; i < GATHER_SLEEP; ++i) __builtin_amdgcn_s_sleep(8); }
    v4u G[16];
    unsigned it = 0;
    for (;; ++it) {
      AGAIN();
      bool ok = true;
#pragma unroll
      for (int i = 0; i < 16; ++i) G[i] = ld16<AUX_SC1>(rws, l * 16 + (i & 3) * 1024, WS_QP + h * 16384 + (i >> 2) * 4096);
#pragma unroll
      for (int i = 0; i < 16; ++i) ok &= tagged(G[i], tag);
      if (__all(ok) || it >= SPIN_CAP) break;
    }
    STAMP(5);
    SETSTAMP(6, it);
#pragma unroll
    for (int i = 0; i < 4; ++i) {
      const float v0 = (asf(G[i].x) + asf(G[4 + i].x)) + (asf(G[8 + i].x) + asf(G[12 + i].x));
      const float v1 = (asf(G[i].z) + asf(G[4 + i].z)) + (asf(G[8 + i].z) + asf(G[12 + i].z));
      *(LAS unsigned*)(L + L_Q + (i >> 1) * 640 + (i & 1) * 256 + l * 4) = pk(v0, v1);
    }
    if (l == 0) *(volatile LAS unsigned*)(L + L_TAG) = tag;
    asm volatile("s_waitcnt lgkmcnt(0)" ::: "memory");
    if (l == 0) *(volatile LAS unsigned*)(L + L_FLAG) = 1u;
    STAMP(7);
    SC_PUB(0);
  } else if (wv == 1) {
    __builtin_amdgcn_s_setprio(3);
    if (b >= 224) { __builtin_amdgcn_s_barrier(); return; }
    const int fc = b >> 3, fr = b & 7;
    const unsigned ep = ld4<AUX_SC1>(rws, WS_E, 0);
    const v4u xv = ld16<0>(mk(a.x, D * 2), l * 16, 0);
    const size_t wofs = (size_t)(56 * fr) * 3584 + fc * 128;
    const rsrc_t rg_ = mk((const char*)a.wg + wofs, 56 * 3584), ru_ = mk((const char*)a.wu + wofs, 56 * 3584);
    v4u Wg[7], Wu[7];
#pragma unroll
    for (int i = 0; i < 7; ++i) Wg[i] = ld16<AUX_NT>(rg_, (l >> 3) * 3584 + (l & 7) * 16, i * 8 * 3584);
#pragma unroll
    for (int i = 0; i < 7; ++i) Wu[i] = ld16<AUX_NT>(ru_, (l >> 3) * 3584 + (l & 7) * 16, i * 8 * 3584);
    __builtin_amdgcn_sched_barrier(0);
    __builtin_amdgcn_s_barrier();
    STAMP(1);
    layernorm(xv, l, L + L_XN1);
    const unsigned tag = __builtin_amdgcn_readfirstlane(ep) + 1u;
    STAMP(2);
    const v4u mk8 = diag_mask(n & 7);
    f32x4 ag = {0.f, 0.f, 0.f, 0.f}, au = {0.f, 0.f, 0.f, 0.f};
    const LAS unsigned* xr = (const LAS unsigned*)(L + L_XN1) + 56 * fr + 2 * g + (n >> 3);
#pragma unroll
    for (int i = 0; i < 7; ++i) { const v4u A = diag(xr[8 * i], mk8); ag = mfma(A, Wg[i], ag); au = mfma(A, Wu[i], au); }
    STAMP(3);
    st16(fold2(ag, au, lo8, tag), rws, (n >> 3) * 512 + colp * 8, WS_FP + fc * 8192 + fr * 1024);
    STAMP(4);
    const rsrc_t rd_ = mk((const char*)a.wd + (size_t)(8 * b) * 896, 8 * 896);
    v4u Wd[7];
#pragma unroll
    for (int i = 0; i < 7; ++i) Wd[i] = ld16<AUX_NT>(rd_, (l >> 3) * 896 + (l & 7) * 16 + i * 128, 0);
    __builtin_amdgcn_sched_barrier(0);
    if (GATHER_SLEEP) { for (int i = 0; i < GATHER_SLEEP; ++i) __builtin_amdgcn_s_sleep(8); }
    v4u G;
    unsigned it = 0;
    for (;; ++it) {
      AGAIN();
      G = ld16<AUX_SC1>(rws, (n & 7) * 1024 + (n >> 3) * 512 + g * 16, WS_FP + fc * 8192 + fr * 64);
      if (__all(tagged(G, tag)) || it >= SPIN_CAP) break;
    }
    STAMP(5);
    SETSTAMP(6, it);
    const float v0 = sum8(asf(G.x)), v1 = sum8(asf(G.z));
    const float own = lo8 ? v0 : v1, rcv = dpp<ROR8>(lo8 ? v1 : v0);
    const float gt = rbf(lo8 ? own : rcv), up = rbf(lo8 ? rcv : own);
    const float u2 = -2.0f * 0.7978845608028654f * 1.4426950408889634f * fmaf(0.044715f * gt * gt, gt, gt);
    const float ge = rbf(gt * __builtin_amdgcn_rcpf(1.0f + __builtin_amdgcn_exp2f(u2)));
    const float hv = ge * up;
    const v4u A = diag(pk(hv, hv), mk8);
    const f32x4 z = {0.f, 0.f, 0.f, 0.f};
    f32x4 F[7];
#pragma unroll
    for (int i = 0; i < 7; ++i) F[i] = mfma(A, Wd[i], z);
#pragma unroll
    for (int i = 0; i < 3; ++i) st16(fold2(F[2 * i], F[2 * i + 1], lo8, tag), rws, (n >> 3) * 512 + colp * 8 + i * 1024, WS_FS + b * 3584);
    st16(fold1(F[6], tag), rws, lo8 ? colp * 8 + 3072 : OOB, WS_FS + b * 3584);
    STAMP(7);
  } else {
    const int w = wv - 2;
    const size_t kvofs = ((size_t)h * SK + (SK - WIN) + s * 128) * 512;
    const rsrc_t rk = mk((const char*)a.k + kvofs, 65536), rv = mk((const char*)a.v + kvofs, 65536);
    const rsrc_t ro = mk((const char*)a.wo + (size_t)(h * 512 + s * 16) * 896, 16 * 896);
    const int rot = ROT_MODE == 0 ? s : ROT_MODE == 1 ? 0 : s + 2 * h;
#define TILE(T) ((4 * (T) + w + rot) & 15)
    __builtin_amdgcn_s_barrier();
    STAMP(1);
    v4u K[16], V[16], WO[4];
#pragma unroll
    for (int i = 0; i < 16; ++i) K[i] = ld16<AUX_NT>(rk, (n & 7) * 512 + (n >> 3) * 64 + g * 16 + (i & 3) * 128, TILE(i >> 2) * 4096);
#pragma unroll
    for (int i = 0; i < 16; ++i) V[i] = ld16<AUX_NT>(rv, TILE(2 * (g >> 1) + (i >> 3)) * 4096 + (g & 1) * 2048 + n * 16 + ((i >> 1) & 3) * 512 + (i & 1) * 256, 0);
#pragma unroll
    for (int i = 0; i < 4; ++i) WO[i] = ld16<AUX_NT>(ro, ((w == 3 && i >= 2) ? OOB : (4 * g + (n >> 3)) * 896 + (n & 7) * 16) + (i & 1) * 1792 + (i >> 1) * 128, w * 256);
    __builtin_amdgcn_sched_barrier(0);
    STAMP(2);
    for (unsigned it = 0; *(volatile LAS unsigned*)(L + L_FLAG) == 0u && it < 64 * SPIN_CAP; ++it) __builtin_amdgcn_s_sleep(1);
    AGAIN();
    STAMP(3);
    const unsigned tag = __builtin_amdgcn_readfirstlane(*(volatile LAS unsigned*)(L + L_TAG));
    const int hd = (n >> 2) & 1;
    v4u Q[4];
#pragma unroll
    for (int i = 0; i < 4; ++i) Q[i] = *(const LAS v4u*)(L + L_Q + hd * 640 + (n >> 3) * 64 + g * 16 + i * 128);
    f32x4 S[4];
#pragma unroll
    for (int t = 0; t < 4; ++t) {
      f32x4 c = {0.f, 0.f, 0.f, 0.f};
#pragma unroll
      for (int i = 0; i < 4; ++i) c = mfma(K[4 * t + i], Q[i], c);
      S[t] = c;
    }
    float e[8];
#pragma unroll
    for (int j = 0; j < 8; ++j) {
      auto x = __builtin_amdgcn_permlane32_swap(asu(S[j >> 2][j & 3]), asu(S[2 + (j >> 2)][j & 3]), false, false);
      const float z = asf(lo8 ? x[0] : x[1]);
      e[j] = rbf(z + dpp<ROR8>(z));
    }
    constexpr float C2 = 0.0625f * 1.4426950408889634f;
    float m = fmaxf(fmaxf(fmaxf(e[0], e[1]), fmaxf(e[2], e[3])), fmaxf(fmaxf(e[4], e[5]), fmaxf(e[6], e[7])));
    m = xrow_max(m) * C2;
    float ls = 0.f;
#pragma unroll
    for (int j = 0; j < 8; ++j) { e[j] = __builtin_amdgcn_exp2f(fmaf(e[j], C2, -m)); ls += e[j]; }
    ls = xrow_sum(ls);
    if (l == 0 || l == 4) { f32x2 t2 = {m, ls}; *(LAS f32x2*)(L + L_ML + hd * 32 + w * 8) = t2; }
    asm volatile("s_waitcnt lgkmcnt(0)" ::: "memory");
    STAMP(4);
    __builtin_amdgcn_s_barrier();
    AGAIN();
    const f32x4 r0 = *(const LAS f32x4*)(L + L_ML + hd * 32), r1 = *(const LAS f32x4*)(L + L_ML + hd * 32 + 16);
    const float mb = fmaxf(fmaxf(r0[0], r0[2]), fmaxf(r1[0], r1[2]));
    if (w == 0) {
      const float lb = (r0[1] * __builtin_amdgcn_exp2f(r0[0] - mb) + r0[3] * __builtin_amdgcn_exp2f(r0[2] - mb)) +
                       (r1[1] * __builtin_amdgcn_exp2f(r1[0] - mb) + r1[3] * __builtin_amdgcn_exp2f(r1[2] - mb));
      const v4u rec = {asu(mb), tag, asu(lb), tag};
      st16(rec, rws, (l == 0 || l == 4) ? hd * 16 : OOB, WS_ML + (h * 32 + s) * 32);
    }
    const float fw = __builtin_amdgcn_exp2f(m - mb);
    const v4u mkv = diag_mask(4 * (n >> 3) + (n & 3));
    v4u A[8];
#pragma unroll
    for (int j = 0; j < 8; ++j) { const float p = e[j] * fw; A[j] = diag(pk(p, p), mkv); }
    __builtin_amdgcn_sched_barrier(0);
    STAMP(5);
    f32x4 o0 = {0.f, 0.f, 0.f, 0.f}, o1 = {0.f, 0.f, 0.f, 0.f};
#pragma unroll
    for (int j = 0; j < 8; ++j) { o0 = mfma(A[j], V[2 * j], o0); o1 = mfma(A[j], V[2 * j + 1], o1); __builtin_amdgcn_sched_barrier(0); }
    *(LAS f32x4*)(L + L_O + w * 2048 + l * 16) = o0;
    *(LAS f32x4*)(L + L_O + w * 2048 + 1024 + l * 16) = o1;
    asm volatile("s_waitcnt lgkmcnt(0)" ::: "memory");
    STAMP(6);
    __builtin_amdgcn_s_barrier();
    AGAIN();
    if (w < 2) {
      const LAS f32x4* p = (const LAS f32x4*)(L + L_O + w * 1024 + l * 16);
      const f32x4 t = (p[0] + p[128]) + (p[256] + p[384]);
      const v4u rec = {pk(t[0], t[1]), tag, pk(t[2], t[3]), tag};
      st16(rec, rws, (g & 1) * 1024 + w * 512 + n * 32 + (g >> 1) * 16, WS_OP + (h * 32 + s) * 2048);
    }
    STAMP(7);
    const int fc = 16 * (s >> 1) + 2 * h + (s & 1);
    const bool fin = (w == 3) && (s < 8) && (fc < 56);
    float wg0, wg1;
    {
      v4u M0, M1;
      for (unsigned it = 0;; ++it) {
        AGAIN();
        M0 = ld16<AUX_SC1>(rws, n * 32, WS_ML + h * 1024 + (s >> 4) * 16);
        M1 = ld16<AUX_SC1>(rws, n * 32 + 512, WS_ML + h * 1024 + (s >> 4) * 16);
        if (__all(tagged(M0, tag) & tagged(M1, tag)) || it >= SPIN_CAP) break;
      }
      const float M = max16(fmaxf(asf(M0.x), asf(M1.x)));
      wg0 = __builtin_amdgcn_exp2f(asf(M0.x) - M); wg1 = __builtin_amdgcn_exp2f(asf(M1.x) - M);
      const float inv = __builtin_amdgcn_rcpf(sum16(fmaf(wg0, asf(M0.z), wg1 * asf(M1.z))));
      wg0 *= inv; wg1 *= inv;
    }
    float gate0 = 0.f, gate1 = 0.f; unsigned xo = 0;
    if (fin) {
      xo = ld4<0>(mk(a.x, D * 2), fc * 16 + g * 4, 0);
      v4u G[14];
      for (unsigned it = 0;; ++it) {
        AGAIN();
        bool ok = true;
#pragma unroll
        for (int i = 0; i < 14; ++i) G[i] = ld16<AUX_SC1>(rws, n * 3584 + g * 16, WS_FS + fc * 64 + i * 16 * 3584);
#pragma unroll
        for (int i = 0; i < 14; ++i) ok &= tagged(G[i], tag);
        if (__all(ok) || it >= SPIN_CAP) break;
      }
      float f0 = 0.f, f1 = 0.f;
#pragma unroll
      for (int i = 0; i < 14; ++i) { f0 += asf(G[i].x); f1 += asf(G[i].z); }
      f0 = rbf(sum16(f0)); f1 = rbf(sum16(f1));
      gate0 = rbf(f0 * __builtin_amdgcn_rcpf(1.0f + __builtin_amdgcn_exp2f(-1.4426950408889634f * f0)));
      gate1 = rbf(f1 * __builtin_amdgcn_rcpf(1.0f + __builtin_amdgcn_exp2f(-1.4426950408889634f * f1)));
    }
    STAMP(8);
    v4u G0, G1;
    for (unsigned it = 0;; ++it) {
      AGAIN();
      G0 = ld16<AUX_SC1>(rws, n * 2048 + g * 16, WS_OP + h * 65536 + s * 64);
      G1 = ld16<AUX_SC1>(rws, n * 2048 + g * 16, WS_OP + h * 65536 + s * 64 + 32768);
      if (__all(tagged(G0, tag) & tagged(G1, tag)) || it >= SPIN_CAP) break;
    }
    STAMP(9);
    const v4u mk8 = diag_mask(n & 7);
    v4u B0, B1;
    {
      const unsigned sk = lo8 ? 0x01000c0cu : 0x03020c0cu, ss = lo8 ? 0x03020c0cu : 0x01000c0cu;
      auto half = [](unsigned x, unsigned sel) { return asf(__builtin_amdgcn_perm(0u, x, sel)); };
      float k0 = fmaf(wg0, half(G0.x, sk), wg1 * half(G1.x, sk)), s0 = fmaf(wg0, half(G0.x, ss), wg1 * half(G1.x, ss));
      float k1 = fmaf(wg0, half(G0.z, sk), wg1 * half(G1.z, sk)), s1 = fmaf(wg0, half(G0.z, ss), wg1 * half(G1.z, ss));
      k0 = sum8(k0 + dpp<ROR8>(s0)); k1 = sum8(k1 + dpp<ROR8>(s1));
      B0 = diag(pk(k0, k0), mk8); B1 = diag(pk(k1, k1), mk8);
    }
    const f32x4 z = {0.f, 0.f, 0.f, 0.f};
    const f32x4 sa = mfma(B1, WO[1], mfma(B0, WO[0], z)), sb = mfma(B1, WO[3], mfma(B0, WO[2], z));
    st16(fold2(sa, sb, lo8, tag), rws, (w == 3 && !lo8) ? OOB : (n >> 3) * 512 + colp * 8 + w * 1024, WS_SP + b * 3584);
    STAMP(10);
    SC_PUB(wv);
    if (fin) {
      v4u G[16];
      for (unsigned it = 0;; ++it) {
        AGAIN();
        bool ok = true;
#pragma unroll
        for (int i = 0; i < 16; ++i) G[i] = ld16<AUX_SC1>(rws, n * 3584 + g * 16, WS_SP + fc * 64 + i * 16 * 3584);
#pragma unroll
        for (int i = 0; i < 16; ++i) ok &= tagged(G[i], tag);
        if (__all(ok) || it >= SPIN_CAP) break;
      }
      float s0 = 0.f, s1 = 0.f;
#pragma unroll
      for (int i = 0; i < 16; ++i) { s0 += asf(G[i].x); s1 += asf(G[i].z); }
      s0 = rbf(sum16(s0)); s1 = rbf(sum16(s1));
      const unsigned r = pk(asf(xo << 16) + rbf(s0 * gate0), asf(xo & 0xffff0000u) + rbf(s1 * gate1));
      if (b == 0) {
        unsigned long long now0; asm volatile("s_memrealtime %0\n\ts_waitcnt lgkmcnt(0)" : "=s"(now0) :: "memory");
        v4u Dg[4];
        for (unsigned it = 0;; ++it) { AGAIN(); bool ok = true;
          for (int i = 0; i < 4; ++i) { Dg[i] = ld16<AUX_SC1>(rws, l * 16 + i * 1024, WS_DBG); ok &= tagged(Dg[i], tag); }
          if (__all(ok) || it >= SPIN_CAP) break; }
        const unsigned base = (unsigned)sct[0] - 100000u;
        unsigned mn0 = ~0u, mx0 = 0, mn1 = ~0u, mx1 = 0, sm1 = 0;
        for (int i = 0; i < 4; ++i) { unsigned a0 = Dg[i].x - base, a1 = Dg[i].z - base; mn0 = min(mn0, a0); mx0 = max(mx0, a0); mn1 = min(mn1, a1); mx1 = max(mx1, a1); sm1 += a1; }
        for (int o = 1; o < 64; o <<= 1) { mn0 = min(mn0, (unsigned)__shfl_xor((int)mn0, o)); mx0 = max(mx0, (unsigned)__shfl_xor((int)mx0, o)); mn1 = min(mn1, (unsigned)__shfl_xor((int)mn1, o)); mx1 = max(mx1, (unsigned)__shfl_xor((int)mx1, o)); sm1 += (unsigned)__shfl_xor((int)sm1, o); }
        unsigned st = SC_STAT == 0 ? mx0 - mn0 : SC_STAT == 1 ? mx1 - mn0 : SC_STAT == 2 ? mn1 - mn0 : SC_STAT == 3 ? ((unsigned)now0 - base) - mn0 : SC_STAT == 4 ? sm1 / 256 - mn0 : 0u;
        st = __builtin_amdgcn_readfirstlane(st);
        if (st > 5000u) st = 5000u;
        for (;;) { unsigned long long t; asm volatile("s_memrealtime %0\n\ts_waitcnt lgkmcnt(0)" : "=s"(t) :: "memory"); if (t - now0 >= 100ull + 4ull * st) break; }
      }
      __builtin_amdgcn_raw_buffer_store_b32(r, mk(a.out, D * 2), n == 0 ? fc * 16 + g * 4 : (int)OOB, 0, 0);
      if (b == 0) __builtin_amdgcn_raw_buffer_store_b32(tag, rws, l == 0 ? (int)WS_E : (int)OOB, 0, AUX_SC1);
      STAMP(11);
    }
  }
}

extern "C" void kernel_launch(void* const* d_in, const int* in_sizes, int n_in, void* d_out, int out_size, void* d_ws, size_t ws_size,
                              hipStream_t stream) {
  Args a;
  a.x = d_in[0]; a.k = d_in[1]; a.v = d_in[2]; a.wq = d_in[3]; a.wo = d_in[4];
  a.wg = d_in[6]; a.wu = d_in[7]; a.wd = d_in[8]; a.out = d_out; a.ws = d_ws;
  (void)hipFuncSetAttribute((const void*)fused_decode, hipFuncAttributeMaxDynamicSharedMemorySize, LDS_BYTES);
  hipLaunchKernelGGL(fused_decode, dim3(256), dim3(384), LDS_BYTES, stream, a);
}
```

```cpp
#define SC_STAT 9
#define SC_WAVE 2
#define SC_AT 1
#include <hip/hip_runtime.h>
#include <hip/hip_bf16.h>

typedef unsigned v4u __attribute__((ext_vector_type(4)));
typedef float f32x4 __attribute__((ext_vector_type(4)));
typedef float f32x2 __attribute__((ext_vector_type(2)));
typedef short bf16x8 __attribute__((ext_vector_type(8)));
typedef __bf16 bf16x2_t __attribute__((ext_vector_type(2)));
#define LAS __attribute__((address_space(3)))
typedef __amdgpu_buffer_rsrc_t rsrc_t;

constexpr int D = 448, SK = 32768, WIN = 4096, DH = 256, FF = 1792;
constexpr int AUX_NT = 2, AUX_SC1 = 16;
constexpr unsigned OOB = 0x7fffff00u;
constexpr unsigned WS_E = 0, WS_QP = 4096, WS_FP = WS_QP + 8 * 4 * 4096, WS_ML = WS_FP + 28 * 8192, WS_OP = WS_ML + 8192,
                   WS_SP = WS_OP + 256 * 2048, WS_FS = 2u << 20, WS_DBG = WS_FS + 224 * 3584, WS_END = WS_DBG + 4096;
static_assert(WS_SP + 256 * 3584 <= WS_FS, "ws map");
constexpr unsigned SPIN_CAP = 1u << 16;
constexpr int L_FLAG = 0, L_TAG = 4, L_ML = 64, L_Q = 128, L_XN0 = L_Q + 1280, L_XN1 = L_XN0 + 1792, L_O = L_XN1 + 1792, L_END = L_O + 8192;
constexpr int LDS_BYTES = 84 * 1024;

#define ROT_MODE 0
#define GATHER_SLEEP 0

#define STAMP(i) do { if ((i) == 0 || ((i) == SC_AT)) { __builtin_amdgcn_sched_barrier(0); asm volatile("s_memrealtime %0\n\ts_waitcnt lgkmcnt(0)" : "=s"(sct[(i) == 0 ? 0 : 1]) :: "memory"); __builtin_amdgcn_sched_barrier(0); } } while (0)
#define SETSTAMP(i, v)
#define SC_PUB(W) do { if ((W) == SC_WAVE) { v4u r_ = {(unsigned)sct[0], tag, (unsigned)sct[1], tag}; st16(r_, rws, l == 0 ? b * 16 : OOB, WS_DBG); } } while (0)

struct Args {
  const void *x, *k, *v, *wq, *wo, *wg, *wu, *wd;
  void *out, *ws;
};

__device__ __forceinline__ rsrc_t mk(const void* p, unsigned bytes) { return __builtin_amdgcn_make_buffer_rsrc((void*)p, (short)0, (int)bytes, 0x00020000); }
template <int AUX> __device__ __forceinline__ v4u ld16(rsrc_t r, unsigned vo, unsigned so) { return __builtin_amdgcn_raw_buffer_load_b128(r, (int)vo, (int)so, AUX); }
template <int AUX> __device__ __forceinline__ unsigned ld4(rsrc_t r, unsigned vo, unsigned so) { return __builtin_amdgcn_raw_buffer_load_b32(r, (int)vo, (int)so, AUX); }
__device__ __forceinline__ void st16(v4u v, rsrc_t r, unsigned vo, unsigned so) { __builtin_amdgcn_raw_buffer_store_b128(v, r, (int)vo, (int)so, AUX_SC1); }
__device__ __forceinline__ f32x4 mfma(v4u a, v4u b, f32x4 c) {
  return __builtin_amdgcn_mfma_f32_16x16x32_bf16(__builtin_bit_cast(bf16x8, a), __builtin_bit_cast(bf16x8, b), c, 0, 0, 0);
}
__device__ __forceinline__ float asf(unsigned u) { return __builtin_bit_cast(float, u); }
__device__ __forceinline__ unsigned asu(float f) { return __builtin_bit_cast(unsigned, f); }
template <int CTRL> __device__ __forceinline__ float dpp(float x) { return asf((unsigned)__builtin_amdgcn_mov_dpp((int)asu(x), CTRL, 0xf, 0xf, true)); }
constexpr int XOR1 = 0xB1, XOR2 = 0x4E, HMIR = 0x141, MIR = 0x140, ROR8 = 0x128, ROR4 = 0x124;
__device__ __forceinline__ unsigned pk(float lo, float hi) {
  f32x2 v = {lo, hi};
  return __builtin_bit_cast(unsigned, __builtin_convertvector(v, bf16x2_t));
}
__device__ __forceinline__ float rbf(float f) { return asf(pk(f, f) & 0xffff0000u); }
__device__ __forceinline__ float sum8(float x) { x += dpp<XOR1>(x); x += dpp<XOR2>(x); x += dpp<HMIR>(x); return x; }
__device__ __forceinline__ float sum16(float x) { x = sum8(x); x += dpp<ROR8>(x); return x; }
__device__ __forceinline__ float max16(float x) { x = fmaxf(x, dpp<XOR1>(x)); x = fmaxf(x, dpp<XOR2>(x)); x = fmaxf(x, dpp<HMIR>(x)); x = fmaxf(x, dpp<ROR8>(x)); return x; }
__device__ __forceinline__ float xrow_sum(float x) {
  auto s = __builtin_amdgcn_permlane16_swap(asu(x), asu(x), false, false); x = asf(s[0]) + asf(s[1]);
  auto t = __builtin_amdgcn_permlane32_swap(asu(x), asu(x), false, false); return asf(t[0]) + asf(t[1]);
}
__device__ __forceinline__ float xrow_max(float x) {
  auto s = __builtin_amdgcn_permlane16_swap(asu(x), asu(x), false, false); x = fmaxf(asf(s[0]), asf(s[1]));
  auto t = __builtin_amdgcn_permlane32_swap(asu(x), asu(x), false, false); return fmaxf(asf(t[0]), asf(t[1]));
}
__device__ __forceinline__ v4u diag_mask(int jj) {
  const unsigned hm = (jj & 1) ? 0xffff0000u : 0x0000ffffu; const int d = jj >> 1;
  v4u m = {d == 0 ? hm : 0u, d == 1 ? hm : 0u, d == 2 ? hm : 0u, d == 3 ? hm : 0u};
  return m;
}
__device__ __forceinline__ v4u diag(unsigned dup, v4u m) { v4u a = {dup & m.x, dup & m.y, dup & m.z, dup & m.w}; return a; }
__device__ __forceinline__ v4u fold2(f32x4 A, f32x4 B, bool lo8, unsigned tag) {
  auto a0 = __builtin_amdgcn_permlane32_swap(asu(A[0]), asu(A[2]), false, false);
  auto a1 = __builtin_amdgcn_permlane32_swap(asu(A[1]), asu(A[3]), false, false);
  auto b0 = __builtin_amdgcn_permlane32_swap(asu(B[0]), asu(B[2]), false, false);
  auto b1 = __builtin_amdgcn_permlane32_swap(asu(B[1]), asu(B[3]), false, false);
  const float s0 = asf(lo8 ? a0[0] : b0[1]) + dpp<ROR8>(asf(lo8 ? b0[0] : a0[1]));
  const float s1 = asf(lo8 ? a1[0] : b1[1]) + dpp<ROR8>(asf(lo8 ? b1[0] : a1[1]));
  v4u r = {asu(s0), tag, asu(s1), tag};
  return r;
}
__device__ __forceinline__ v4u fold1(f32x4 A, unsigned tag) {
  auto a0 = __builtin_amdgcn_permlane32_swap(asu(A[0]), asu(A[2]), false, false);
  auto a1 = __builtin_amdgcn_permlane32_swap(asu(A[1]), asu(A[3]), false, false);
  v4u r = {asu(asf(a0[0]) + dpp<ROR8>(asf(a0[1]))), tag, asu(asf(a1[0]) + dpp<ROR8>(asf(a1[1]))), tag};
  return r;
}
__device__ __forceinline__ bool tagged(v4u g, unsigned tag) { return (g.y == tag) & (g.w == tag); }
#define AGAIN() asm volatile("" ::: "memory")

__device__ __forceinline__ void layernorm(v4u xv, int l, LAS unsigned char* dst) {
  float e[8];
#pragma unroll
  for (int i = 0; i < 4; ++i) { e[2 * i] = asf(xv[i] << 16); e[2 * i + 1] = asf(xv[i] & 0xffff0000u); }
  float s = ((e[0] + e[1]) + (e[2] + e[3])) + ((e[4] + e[5]) + (e[6] + e[7]));
  const float mean = xrow_sum(sum16(s)) * (1.0f / D);
  float q = 0.f;
#pragma unroll
  for (int i = 0; i < 8; ++i) { const float d = e[i] - mean; q = fmaf(d, d, q); }
  q = l < 56 ? q : 0.f;
  const float var = rbf(xrow_sum(sum16(q)) * (1.0f / D));
  const float den = rbf(__builtin_sqrtf(rbf(var + 1e-5f)));
  const float inv = 1.0f / den, mb = rbf(mean);
  v4u o0, o1;
#pragma unroll
  for (int i = 0; i < 4; ++i) { const float a = rbf(e[i] - mb) * inv, b = rbf(e[4 + i] - mb) * inv; o0[i] = pk(a, a); o1[i] = pk(b, b); }
  if (l < 56) { *(LAS v4u*)(dst + l * 32) = o0; *(LAS v4u*)(dst + l * 32 + 16) = o1; }
}

__global__ void __launch_bounds__(384, 2) fused_decode(Args a) {
  extern __shared__ __attribute__((aligned(16))) unsigned char smem[];
  LAS unsigned char* L = (LAS unsigned char*)smem;
  unsigned long long sct[2] = {0, 0};
  STAMP(0);
  const int b = blockIdx.x, h = b & 7, s = b >> 3;
  const int wv = __builtin_amdgcn_readfirstlane(threadIdx.x >> 6), l = threadIdx.x & 63, g = l >> 4, n = l & 15;
  const bool lo8 = n < 8;
  const rsrc_t rws = mk(a.ws, WS_END);
  const unsigned colp = 8 * (n & 7) + 4 * (g & 1) + 2 * (l >> 5);

  if (wv == 0) {
    __builtin_amdgcn_s_setprio(3);
    const int rg = s & 3, cg = s >> 2;
    if (l == 0) *(volatile LAS unsigned*)(L + L_FLAG) = 0u;
    const unsigned ep = ld4<AUX_SC1>(rws, WS_E, 0);
    const v4u xv = ld16<0>(mk(a.x, D * 2), l * 16, 0);
    const rsrc_t rw = mk((const char*)a.wq + (size_t)(112 * rg) * 8192 + h * 1024 + cg * 128, 112 * 8192);
    v4u W[14];
#pragma unroll
    for (int i = 0; i < 14; ++i) W[i] = ld16<AUX_NT>(rw, (l >> 3) * 8192 + (l & 7) * 16, i * 65536);
    __builtin_amdgcn_sched_barrier(0);
    asm volatile("s_waitcnt lgkmcnt(0)" ::: "memory");
    __builtin_amdgcn_s_barrier();
    STAMP(1);
    layernorm(xv, l, L + L_XN0);
    const unsigned tag = __builtin_amdgcn_readfirstlane(ep) + 1u;
    STAMP(2);
    const v4u mk8 = diag_mask(n & 7);
    f32x4 acc = {0.f, 0.f, 0.f, 0.f};
    const LAS unsigned* xr = (const LAS unsigned*)(L + L_XN0) + 112 * rg + 2 * g + (n >> 3);
#pragma unroll
    for (int i = 0; i < 14; ++i) acc = mfma(diag(xr[8 * i], mk8), W[i], acc);
    STAMP(3);
    st16(fold1(acc, tag), rws, lo8 ? (64 * cg + colp) * 8 : OOB, WS_QP + (h * 4 + rg) * 4096);
    STAMP(4);
    if (GATHER_SLEEP) { for (int i = 0; i < GATHER_SLEEP; ++i) __builtin_amdgcn_s_sleep(8); }
    v4u G[16];
    unsigned it = 0;
    for (;; ++it) {
      AGAIN();
      bool ok = true;
#pragma unroll
      for (int i = 0; i < 16; ++i) G[i] = ld16<AUX_SC1>(rws, l * 16 + (i & 3) * 1024, WS_QP + h * 16384 + (i >> 2) * 4096);
#pragma unroll
      for (int i = 0; i < 16; ++i) ok &= tagged(G[i], tag);
      if (__all(ok) || it >= SPIN_CAP) break;
    }
    STAMP(5);
    SETSTAMP(6, it);
#pragma unroll
    for (int i = 0; i < 4; ++i) {
      const float v0 = (asf(G[i].x) + asf(G[4 + i].x)) + (asf(G[8 + i].x) + asf(G[12 + i].x));
      const float v1 = (asf(G[i].z) + asf(G[4 + i].z)) + (asf(G[8 + i].z) + asf(G[12 + i].z));
      *(LAS unsigned*)(L + L_Q + (i >> 1) * 640 + (i & 1) * 256 + l * 4) = pk(v0, v1);
    }
    if (l == 0) *(volatile LAS unsigned*)(L + L_TAG) = tag;
    asm volatile("s_waitcnt lgkmcnt(0)" ::: "memory");
    if (l == 0) *(volatile LAS unsigned*)(L + L_FLAG) = 1u;
    STAMP(7);
    SC_PUB(0);
  } else if (wv == 1) {
    __builtin_amdgcn_s_setprio(3);
    if (b >= 224) { __builtin_amdgcn_s_barrier(); return; }
    const int fc = b >> 3, fr = b & 7;
    const unsigned ep = ld4<AUX_SC1>(rws, WS_E, 0);
    const v4u xv = ld16<0>(mk(a.x, D * 2), l * 16, 0);
    const size_t wofs = (size_t)(56 * fr) * 3584 + fc * 128;
    const rsrc_t rg_ = mk((const char*)a.wg + wofs, 56 * 3584), ru_ = mk((const char*)a.wu + wofs, 56 * 3584);
    v4u Wg[7], Wu[7];
#pragma unroll
    for (int i = 0; i < 7; ++i) Wg[i] = ld16<AUX_NT>(rg_, (l >> 3) * 3584 + (l & 7) * 16, i * 8 * 3584);
#pragma unroll
    for (int i = 0; i < 7; ++i) Wu[i] = ld16<AUX_NT>(ru_, (l >> 3) * 3584 + (l & 7) * 16, i * 8 * 3584);
    __builtin_amdgcn_sched_barrier(0);
    __builtin_amdgcn_s_barrier();
    STAMP(1);
    layernorm(xv, l, L + L_XN1);
    const unsigned tag = __builtin_amdgcn_readfirstlane(ep) + 1u;
    STAMP(2);
    const v4u mk8 = diag_mask(n & 7);
    f32x4 ag = {0.f, 0.f, 0.f, 0.f}, au = {0.f, 0.f, 0.f, 0.f};
    const LAS unsigned* xr = (const LAS unsigned*)(L + L_XN1) + 56 * fr + 2 * g + (n >> 3);
#pragma unroll
    for (int i = 0; i < 7; ++i) { const v4u A = diag(xr[8 * i], mk8); ag = mfma(A, Wg[i], ag); au = mfma(A, Wu[i], au); }
    STAMP(3);
    st16(fold2(ag, au, lo8, tag), rws, (n >> 3) * 512 + colp * 8, WS_FP + fc * 8192 + fr * 1024);
    STAMP(4);
    const rsrc_t rd_ = mk((const char*)a.wd + (size_t)(8 * b) * 896, 8 * 896);
    v4u Wd[7];
#pragma unroll
    for (int i = 0; i < 7; ++i) Wd[i] = ld16<AUX_NT>(rd_, (l >> 3) * 896 + (l & 7) * 16 + i * 128, 0);
    __builtin_amdgcn_sched_barrier(0);
    if (GATHER_SLEEP) { for (int i = 0; i < GATHER_SLEEP; ++i) __builtin_amdgcn_s_sleep(8); }
    v4u G;
    unsigned it = 0;
    for (;; ++it) {
      AGAIN();
      G = ld16<AUX_SC1>(rws, (n & 7) * 1024 + (n >> 3) * 512 + g * 16, WS_FP + fc * 8192 + fr * 64);
      if (__all(tagged(G, tag)) || it >= SPIN_CAP) break;
    }
    STAMP(5);
    SETSTAMP(6, it);
    const float v0 = sum8(asf(G.x)), v1 = sum8(asf(G.z));
    const float own = lo8 ? v0 : v1, rcv = dpp<ROR8>(lo8 ? v1 : v0);
    const float gt = rbf(lo8 ? own : rcv), up = rbf(lo8 ? rcv : own);
    const float u2 = -2.0f * 0.7978845608028654f * 1.4426950408889634f * fmaf(0.044715f * gt * gt, gt, gt);
    const float ge = rbf(gt * __builtin_amdgcn_rcpf(1.0f + __builtin_amdgcn_exp2f(u2)));
    const float hv = ge * up;
    const v4u A = diag(pk(hv, hv), mk8);
    const f32x4 z = {0.f, 0.f, 0.f, 0.f};
    f32x4 F[7];
#pragma unroll
    for (int i = 0; i < 7; ++i) F[i] = mfma(A, Wd[i], z);
#pragma unroll
    for (int i = 0; i < 3; ++i) st16(fold2(F[2 * i], F[2 * i + 1], lo8, tag), rws, (n >> 3) * 512 + colp * 8 + i * 1024, WS_FS + b * 3584);
    st16(fold1(F[6], tag), rws, lo8 ? colp * 8 + 3072 : OOB, WS_FS + b * 3584);
    STAMP(7);
  } else {
    const int w = wv - 2;
    const size_t kvofs = ((size_t)h * SK + (SK - WIN) + s * 128) * 512;
    const rsrc_t rk = mk((const char*)a.k + kvofs, 65536), rv = mk((const char*)a.v + kvofs, 65536);
    const rsrc_t ro = mk((const char*)a.wo + (size_t)(h * 512 + s * 16) * 896, 16 * 896);
    const int rot = ROT_MODE == 0 ? s : ROT_MODE == 1 ? 0 : s + 2 * h;
#define TILE(T) ((4 * (T) + w + rot) & 15)
    __builtin_amdgcn_s_barrier();
    STAMP(1);
    v4u K[16], V[16], WO[4];
#pragma unroll
    for (int i = 0; i < 16; ++i) K[i] = ld16<AUX_NT>(rk, (n & 7) * 512 + (n >> 3) * 64 + g * 16 + (i & 3) * 128, TILE(i >> 2) * 4096);
#pragma unroll
    for (int i = 0; i < 16; ++i) V[i] = ld16<AUX_NT>(rv, TILE(2 * (g >> 1) + (i >> 3)) * 4096 + (g & 1) * 2048 + n * 16 + ((i >> 1) & 3) * 512 + (i & 1) * 256, 0);
#pragma unroll
    for (int i = 0; i < 4; ++i) WO[i] = ld16<AUX_NT>(ro, ((w == 3 && i >= 2) ? OOB : (4 * g + (n >> 3)) * 896 + (n & 7) * 16) + (i & 1) * 1792 + (i >> 1) * 128, w * 256);
    __builtin_amdgcn_sched_barrier(0);
    STAMP(2);
    for (unsigned it = 0; *(volatile LAS unsigned*)(L + L_FLAG) == 0u && it < 64 * SPIN_CAP; ++it) __builtin_amdgcn_s_sleep(1);
    AGAIN();
    STAMP(3);
    const unsigned tag = __builtin_amdgcn_readfirstlane(*(volatile LAS unsigned*)(L + L_TAG));
    const int hd = (n >> 2) & 1;
    v4u Q[4];
#pragma unroll
    for (int i = 0; i < 4; ++i) Q[i] = *(const LAS v4u*)(L + L_Q + hd * 640 + (n >> 3) * 64 + g * 16 + i * 128);
    f32x4 S[4];
#pragma unroll
    for (int t = 0; t < 4; ++t) {
      f32x4 c = {0.f, 0.f, 0.f, 0.f};
#pragma unroll
      for (int i = 0; i < 4; ++i) c = mfma(K[4 * t + i], Q[i], c);
      S[t] = c;
    }
    float e[8];
#pragma unroll
    for (int j = 0; j < 8; ++j) {
      auto x = __builtin_amdgcn_permlane32_swap(asu(S[j >> 2][j & 3]), asu(S[2 + (j >> 2)][j & 3]), false, false);
      const float z = asf(lo8 ? x[0] : x[1]);
      e[j] = rbf(z + dpp<ROR8>(z));
    }
    constexpr float C2 = 0.0625f * 1.4426950408889634f;
    float m = fmaxf(fmaxf(fmaxf(e[0], e[1]), fmaxf(e[2], e[3])), fmaxf(fmaxf(e[4], e[5]), fmaxf(e[6], e[7])));
    m = xrow_max(m) * C2;
    float ls = 0.f;
#pragma unroll
    for (int j = 0; j < 8; ++j) { e[j] = __builtin_amdgcn_exp2f(fmaf(e[j], C2, -m)); ls += e[j]; }
    ls = xrow_sum(ls);
    if (l == 0 || l == 4) { f32x2 t2 = {m, ls}; *(LAS f32x2*)(L + L_ML + hd * 32 + w * 8) = t2; }
    asm volatile("s_waitcnt lgkmcnt(0)" ::: "memory");
    STAMP(4);
    __builtin_amdgcn_s_barrier();
    AGAIN();
    const f32x4 r0 = *(const LAS f32x4*)(L + L_ML + hd * 32), r1 = *(const LAS f32x4*)(L + L_ML + hd * 32 + 16);
    const float mb = fmaxf(fmaxf(r0[0], r0[2]), fmaxf(r1[0], r1[2]));
    if (w == 0) {
      const float lb = (r0[1] * __builtin_amdgcn_exp2f(r0[0] - mb) + r0[3] * __builtin_amdgcn_exp2f(r0[2] - mb)) +
                       (r1[1] * __builtin_amdgcn_exp2f(r1[0] - mb) + r1[3] * __builtin_amdgcn_exp2f(r1[2] - mb));
      const v4u rec = {asu(mb), tag, asu(lb), tag};
      st16(rec, rws, (l == 0 || l == 4) ? hd * 16 : OOB, WS_ML + (h * 32 + s) * 32);
    }
    const float fw = __builtin_amdgcn_exp2f(m - mb);
    const v4u mkv = diag_mask(4 * (n >> 3) + (n & 3));
    v4u A[8];
#pragma unroll
    for (int j = 0; j < 8; ++j) { const float p = e[j] * fw; A[j] = diag(pk(p, p), mkv); }
    __builtin_amdgcn_sched_barrier(0);
    STAMP(5);
    f32x4 o0 = {0.f, 0.f, 0.f, 0.f}, o1 = {0.f, 0.f, 0.f, 0.f};
#pragma unroll
    for (int j = 0; j < 8; ++j) { o0 = mfma(A[j], V[2 * j], o0); o1 = mfma(A[j], V[2 * j + 1], o1); __builtin_amdgcn_sched_barrier(0); }
    *(LAS f32x4*)(L + L_O + w * 2048 + l * 16) = o0;
    *(LAS f32x4*)(L + L_O + w * 2048 + 1024 + l * 16) = o1;
    asm volatile("s_waitcnt lgkmcnt(0)" ::: "memory");
    STAMP(6);
    __builtin_amdgcn_s_barrier();
    AGAIN();
    if (w < 2) {
      const LAS f32x4* p = (const LAS f32x4*)(L + L_O + w * 1024 + l * 16);
      const f32x4 t = (p[0] + p[128]) + (p[256] + p[384]);
      const v4u rec = {pk(t[0], t[1]), tag, pk(t[2], t[3]), tag};
      st16(rec, rws, (g & 1) * 1024 + w * 512 + n * 32 + (g >> 1) * 16, WS_OP + (h * 32 + s) * 2048);
    }
    STAMP(7);
    const int fc = 16 * (s >> 1) + 2 * h + (s & 1);
    const bool fin = (w == 3) && (s < 8) && (fc < 56);
    float wg0, wg1;
    {
      v4u M0, M1;
      for (unsigned it = 0;; ++it) {
        AGAIN();
        M0 = ld16<AUX_SC1>(rws, n * 32, WS_ML + h * 1024 + (s >> 4) * 16);
        M1 = ld16<AUX_SC1>(rws, n * 32 + 512, WS_ML + h * 1024 + (s >> 4) * 16);
        if (__all(tagged(M0, tag) & tagged(M1, tag)) || it >= SPIN_CAP) break;
      }
      const float M = max16(fmaxf(asf(M0.x), asf(M1.x)));
      wg0 = __builtin_amdgcn_exp2f(asf(M0.x) - M); wg1 = __builtin_amdgcn_exp2f(asf(M1.x) - M);
      const float inv = __builtin_amdgcn_rcpf(sum16(fmaf(wg0, asf(M0.z), wg1 * asf(M1.z))));
      wg0 *= inv; wg1 *= inv;
    }
    float gate0 = 0.f, gate1 = 0.f; unsigned xo = 0;
    if (fin) {
      xo = ld4<0>(mk(a.x, D * 2), fc * 16 + g * 4, 0);
      v4u G[14];
      for (unsigned it = 0;; ++it) {
        AGAIN();
        bool ok = true;
#pragma unroll
        for (int i = 0; i < 14; ++i) G[i] = ld16<AUX_SC1>(rws, n * 3584 + g * 16, WS_FS + fc * 64 + i * 16 * 3584);
#pragma unroll
        for (int i = 0; i < 14; ++i) ok &= tagged(G[i], tag);
        if (__all(ok) || it >= SPIN_CAP) break;
      }
      float f0 = 0.f, f1 = 0.f;
#pragma unroll
      for (int i = 0; i < 14; ++i) { f0 += asf(G[i].x); f1 += asf(G[i].z); }
      f0 = rbf(sum16(f0)); f1 = rbf(sum16(f1));
      gate0 = rbf(f0 * __builtin_amdgcn_rcpf(1.0f + __builtin_amdgcn_exp2f(-1.4426950408889634f * f0)));
      gate1 = rbf(f1 * __builtin_amdgcn_rcpf(1.0f + __builtin_amdgcn_exp2f(-1.4426950408889634f * f1)));
    }
    STAMP(8);
    v4u G0, G1;
    for (unsigned it = 0;; ++it) {
      AGAIN();
      G0 = ld16<AUX_SC1>(rws, n * 2048 + g * 16, WS_OP + h * 65536 + s * 64);
      G1 = ld16<AUX_SC1>(rws, n * 2048 + g * 16, WS_OP + h * 65536 + s * 64 + 32768);
      if (__all(tagged(G0, tag) & tagged(G1, tag)) || it >= SPIN_CAP) break;
    }
    STAMP(9);
    const v4u mk8 = diag_mask(n & 7);
    v4u B0, B1;
    {
      const unsigned sk = lo8 ? 0x01000c0cu : 0x03020c0cu, ss = lo8 ? 0x03020c0cu : 0x01000c0cu;
      auto half = [](unsigned x, unsigned sel) { return asf(__builtin_amdgcn_perm(0u, x, sel)); };
      float k0 = fmaf(wg0, half(G0.x, sk), wg1 * half(G1.x, sk)), s0 = fmaf(wg0, half(G0.x, ss), wg1 * half(G1.x, ss));
      float k1 = fmaf(wg0, half(G0.z, sk), wg1 * half(G1.z, sk)), s1 = fmaf(wg0, half(G0.z, ss), wg1 * half(G1.z, ss));
      k0 = sum8(k0 + dpp<ROR8>(s0)); k1 = sum8(k1 + dpp<ROR8>(s1));
      B0 = diag(pk(k0, k0), mk8); B1 = diag(pk(k1, k1), mk8);
    }
    const f32x4 z = {0.f, 0.f, 0.f, 0.f};
    const f32x4 sa = mfma(B1, WO[1], mfma(B0, WO[0], z)), sb = mfma(B1, WO[3], mfma(B0, WO[2], z));
    st16(fold2(sa, sb, lo8, tag), rws, (w == 3 && !lo8) ? OOB : (n >> 3) * 512 + colp * 8 + w * 1024, WS_SP + b * 3584);
    STAMP(10);
    SC_PUB(wv);
    if (fin) {
      v4u G[16];
      for (unsigned it = 0;; ++it) {
        AGAIN();
        bool ok = true;
#pragma unroll
        for (int i = 0; i < 16; ++i) G[i] = ld16<AUX_SC1>(rws, n * 3584 + g * 16, WS_SP + fc * 64 + i * 16 * 3584);
#pragma unroll
        for (int i = 0; i < 16; ++i) ok &= tagged(G[i], tag);
        if (__all(ok) || it >= SPIN_CAP) break;
      }
      float s0 = 0.f, s1 = 0.f;
#pragma unroll
      for (int i = 0; i < 16; ++i) { s0 += asf(G[i].x); s1 += asf(G[i].z); }
      s0 = rbf(sum16(s0)); s1 = rbf(sum16(s1));
      const unsigned r = pk(asf(xo << 16) + rbf(s0 * gate0), asf(xo & 0xffff0000u) + rbf(s1 * gate1));
      if (b == 0) {
        unsigned long long now0; asm volatile("s_memrealtime %0\n\ts_waitcnt lgkmcnt(0)" : "=s"(now0) :: "memory");
        v4u Dg[4];
        for (unsigned it = 0;; ++it) { AGAIN(); bool ok = true;
          for (int i = 0; i < 4; ++i) { Dg[i] = ld16<AUX_SC1>(rws, l * 16 + i * 1024, WS_DBG); ok &= tagged(Dg[i], tag); }
          if (__all(ok) || it >= SPIN_CAP) break; }
        const unsigned base = (unsigned)sct[0] - 100000u;
        unsigned mn0 = ~0u, mx0 = 0, mn1 = ~0u, mx1 = 0, sm1 = 0;
        for (int i = 0; i < 4; ++i) { unsigned a0 = Dg[i].x - base, a1 = Dg[i].z - base; mn0 = min(mn0, a0); mx0 = max(mx0, a0); mn1 = min(mn1, a1); mx1 = max(mx1, a1); sm1 += a1; }
        for (int o = 1; o < 64; o <<= 1) { mn0 = min(mn0, (unsigned)__shfl_xor((int)mn0, o)); mx0 = max(mx0, (unsigned)__shfl_xor((int)mx0, o)); mn1 = min(mn1, (unsigned)__shfl_xor((int)mn1, o)); mx1 = max(mx1, (unsigned)__shfl_xor((int)mx1, o)); sm1 += (unsigned)__shfl_xor((int)sm1, o); }
        unsigned st = SC_STAT == 0 ? mx0 - mn0 : SC_STAT == 1 ? mx1 - mn0 : SC_STAT == 2 ? mn1 - mn0 : SC_STAT == 3 ? ((unsigned)now0 - base) - mn0 : SC_STAT == 4 ? sm1 / 256 - mn0 : 0u;
        st = __builtin_amdgcn_readfirstlane(st);
        if (st > 5000u) st = 5000u;
        if (SC_STAT == 9) { const unsigned tm = __builtin_amdgcn_readfirstlane(mn0) + base;
          for (;;) { unsigned long long t; asm volatile("s_memrealtime %0\n\ts_waitcnt lgkmcnt(0)" : "=s"(t) :: "memory"); if ((unsigned)t - tm >= 3000u) break; } }
        else
        for (;;) { unsigned long long t; asm volatile("s_memrealtime %0\n\ts_waitcnt lgkmcnt(0)" : "=s"(t) :: "memory"); if (t - now0 >= 100ull + 4ull * st) break; }
      }
      __builtin_amdgcn_raw_buffer_store_b32(r, mk(a.out, D * 2), n == 0 ? fc * 16 + g * 4 : (int)OOB, 0, 0);
      if (b == 0) __builtin_amdgcn_raw_buffer_store_b32(tag, rws, l == 0 ? (int)WS_E : (int)OOB, 0, AUX_SC1);
      STAMP(11);
    }
  }
}

extern "C" void kernel_launch(void* const* d_in, const int* in_sizes, int n_in, void* d_out, int out_size, void* d_ws, size_t ws_size,
                              hipStream_t stream) {
  Args a;
  a.x = d_in[0]; a.k = d_in[1]; a.v = d_in[2]; a.wq = d_in[3]; a.wo = d_in[4];
  a.wg = d_in[6]; a.wu = d_in[7]; a.wd = d_in[8]; a.out = d_out; a.ws = d_ws;
  (void)hipFuncSetAttribute((const void*)fused_decode, hipFuncAttributeMaxDynamicSharedMemorySize, LDS_BYTES);
  hipLaunchKernelGGL(fused_decode, dim3(256), dim3(384), LDS_BYTES, stream, a);
}
```

```cpp
#include <hip/hip_runtime.h>

extern "C" __global__ void __launch_bounds__(576, 3)
fused_decode(const void* x, void* ws, const void* wq, const void* k, const void* wg, const void* wu, const void* v, const void* wo, const void* wd, void* out) {
#if defined(__HIP_DEVICE_COMPILE__) && __HIP_DEVICE_COMPILE__
  asm volatile(
R"ASM(
	; ======== entry (all waves). Everything up to the first loads sits in the first 700 bytes, which arrive with the first instruction fetch.
	; pointers: s[40:41] x, s[28:29] ws, s[8:23] wq k wg wu v wo wd out
	s_load_dwordx2 s[40:41], s[0:1], 0x0
	s_load_dwordx2 s[28:29], s[0:1], 0x8
	s_load_dwordx16 s[8:23], s[0:1], 0x10
	s_getpc_b64 s[50:51]
.Lfd_pc:
	; read the kernel's own code once as data (a quarter per workgroup): later instruction fetches then hit L2 (speed only)
	v_lshrrev_b32_e32 v3, 4, v0
	s_bfe_u32 s47, s2, 0x20003
	v_lshl_or_b32 v3, v3, 2, s47
	v_lshlrev_b32_e32 v3, 7, v3
	v_min_u32_e32 v3, .Lfd_end-.Lfd_pc-4, v3
	global_load_dword v8, v3, s[50:51]
	; s6 wave, v1 lane, s3 kv head, s4 split, s5 wave mod 4; zero the LDS counters; buffer descriptors of x and ws
	v_readfirstlane_b32 s6, v0
	v_and_b32_e32 v1, 63, v0
	v_mov_b32_e32 v10, 0
	v_mov_b32_e32 v11, 0
	v_lshlrev_b32_e32 v3, 3, v1
	v_mov_b32_e32 v9, 0
	ds_write_b64 v3, v[10:11]
	s_lshr_b32 s6, s6, 6
	s_and_b32 s3, s2, 7
	s_lshr_b32 s4, s2, 3
	s_movk_i32 s42, 0x380
	s_mov_b32 s43, 0x20000
	s_mov_b32 s30, 0x1d8000
	s_mov_b32 s31, s43
	s_and_b32 s5, s6, 3
	v_lshrrev_b32_e32 v18, 3, v1
	v_and_b32_e32 v17, 7, v1
	s_cmp_lg_u32 s6, 0
	s_cbranch_scc1 .Lfd_b1
	; ---- wave 0: x, the epoch word and the q-projection tile (rows 112 (s mod 4) .., columns 512 h + 64 (s / 4) ..)
	s_setprio 2
	s_and_b32 s54, s4, 3
	s_mul_i32 s54, s54, 0xe0000
	s_lshl_b32 s55, s3, 10
	s_add_u32 s54, s54, s55
	s_lshr_b32 s55, s4, 2
	s_lshl_b32 s55, s55, 7
	s_add_u32 s54, s54, s55
	v_lshlrev_b32_e32 v17, 4, v17
	v_lshl_or_b32 v17, v18, 13, v17
	s_mov_b32 s53, 0x10000
	s_mov_b32 s57, 0
	s_waitcnt lgkmcnt(0)
	s_add_u32 s58, s8, s54
	s_addc_u32 s59, s9, 0
	s_mov_b64 s[60:61], s[58:59]
.Lfd_tile:
	v_lshlrev_b32_e32 v3, 4, v1
	buffer_load_dwordx4 v[12:15], v3, s[40:43], 0 offen
	buffer_load_dword v16, off, s[28:31], 0 sc1
.Lfd_tile1:
	; 14 loads of 8 rows x 128 bytes (wave 8 enters here: 7 of gate, 7 of up)
	s_cmp_eq_u32 s6, 0
	global_load_dwordx4 v[20:23], v17, s[58:59] nt
	v_add_u32_e32 v17, s53, v17
	global_load_dwordx4 v[24:27], v17, s[58:59] nt
	v_add_u32_e32 v17, s53, v17
	global_load_dwordx4 v[28:31], v17, s[58:59] nt
	v_add_u32_e32 v17, s53, v17
	global_load_dwordx4 v[32:35], v17, s[58:59] nt
	v_add_u32_e32 v17, s53, v17
	global_load_dwordx4 v[36:39], v17, s[58:59] nt
	v_add_u32_e32 v17, s53, v17
	global_load_dwordx4 v[40:43], v17, s[58:59] nt
	v_add_u32_e32 v17, s53, v17
	global_load_dwordx4 v[44:47], v17, s[58:59] nt
	v_add_u32_e32 v17, s53, v17
	v_subrev_u32_e32 v17, s57, v17
	global_load_dwordx4 v[48:51], v17, s[60:61] nt
	v_add_u32_e32 v17, s53, v17
	global_load_dwordx4 v[52:55], v17, s[60:61] nt
	v_add_u32_e32 v17, s53, v17
	global_load_dwordx4 v[56:59], v17, s[60:61] nt
	v_add_u32_e32 v17, s53, v17
	global_load_dwordx4 v[60:63], v17, s[60:61] nt
	v_add_u32_e32 v17, s53, v17
	global_load_dwordx4 v[64:67], v17, s[60:61] nt
	v_add_u32_e32 v17, s53, v17
	global_load_dwordx4 v[68:71], v17, s[60:61] nt
	v_add_u32_e32 v17, s53, v17
	global_load_dwordx4 v[72:75], v17, s[60:61] nt
	s_cbranch_scc0 .Lfd_ln
	s_barrier
	s_branch .Lfd_ln
.Lfd_b1:
	; ---- all other waves; s54 = byte offset of this workgroup's 128 keys in K and V
	s_lshl_b32 s54, s3, 8
	s_add_u32 s54, s54, s4
	s_addk_i32 s54, 0xe0
	s_lshl_b32 s54, s54, 16
	; waves 2, 3: one timed read each from an unused line in the two 8-KB halves of ws; the quicker half is on this XCD's side of the package (speed only)
	s_xor_b32 s57, s6, s4
	s_and_b32 s57, s57, 1
	s_lshl_b32 s55, s3, 2
	s_add_u32 s55, s55, s57
	s_lshl_b32 s55, s55, 6
	s_add_u32 s55, s55, s4
	s_lshl_b32 s55, s55, 7
	s_add_u32 s55, s55, 0x198000
	s_and_b32 s56, s6, 6
	s_cmp_lg_u32 s56, 2
	s_waitcnt lgkmcnt(0)
	s_cbranch_scc1 .Lfd_noprobe
	s_memtime s[58:59]
	s_load_dword s33, s[28:29], s55
	s_barrier
	s_waitcnt lgkmcnt(0)
	s_memtime s[60:61]
	s_branch .Lfd_b23
.Lfd_noprobe:
	; the barrier keeps the other waves' loads behind wave 0's and orders the LDS clear
	s_barrier
	s_bitcmp1_b32 s6, 2
	s_cbranch_scc1 .Lfd_a0
	s_cmp_lg_u32 s6, 8
	s_cbranch_scc1 .Lfd_b23
	s_cmp_lt_u32 s4, 28
	s_cbranch_scc0 .Lfd_ftouch
	; ---- wave 8 (s < 28): gate / up tiles, rows 56 h .., columns 64 s ..
	s_setprio 2
	s_mul_i32 s54, s3, 0x31000
	s_lshl_b32 s55, s4, 7
	s_add_u32 s54, s54, s55
	s_movk_i32 s53, 0xe00
	v_lshlrev_b32_e32 v17, 4, v17
	v_mad_u32_u24 v17, v18, s53, v17
	s_movk_i32 s53, 0x7000
	s_mul_i32 s57, s53, 7
	s_add_u32 s58, s12, s54
	s_addc_u32 s59, s13, 0
	s_add_u32 s60, s14, s54
	s_addc_u32 s61, s15, 0
	s_branch .Lfd_tile1
.Lfd_a0:
	; ---- waves 4..7: K. A load = 8 keys x 128 bytes; lane (g, n): key n mod 8, bytes 64 (n / 8) + 16 g. Tiles are rotated by 3 s + 7 h (spreads the channels)
	s_setprio 3
	v_lshlrev_b32_e32 v17, 9, v17
	v_and_b32_e32 v3, 8, v1
	v_lshl_or_b32 v17, v3, 3, v17
	v_and_or_b32 v17, v1, 48, v17
	s_mul_i32 s7, s4, 3
	s_mul_i32 s52, s3, 7
	s_add_u32 s7, s7, s52
	s_lshl2_add_u32 s52, s5, s7
	s_lshl_b32 s52, s52, 12
	s_and_b32 s55, s52, 0xf000
	v_or_b32_e32 v18, s55, v17
	s_add_u32 s58, s10, s54
	s_addc_u32 s59, s11, 0
	global_load_dwordx4 v[20:23], v18, s[58:59] offset:0 nt
	global_load_dwordx4 v[24:27], v18, s[58:59] offset:128 nt
	global_load_dwordx4 v[28:31], v18, s[58:59] offset:256 nt
	global_load_dwordx4 v[32:35], v18, s[58:59] offset:384 nt
	s_addk_i32 s52, 0x1000
	s_and_b32 s55, s52, 0xf000
	v_or_b32_e32 v18, s55, v17
	global_load_dwordx4 v[36:39], v18, s[58:59] offset:0 nt
	s_branch .Lfd_a1
.Lfd_b23:
	; ---- waves 1..3: probe result to LDS; waves 2, 3 clear the table of P.V operands
	s_waitcnt lgkmcnt(0)
	s_cmp_eq_u32 s6, 1
	s_cbranch_scc1 .Lfd_np1
	s_sub_u32 s47, s60, s58
	s_lshl_b32 s57, s57, 2
	v_mov_b32_e32 v3, s47
	v_mov_b32_e32 v2, s57
	ds_write_b32 v2, v3 offset:48
.Lfd_np1:
	v_and_b32_e32 v3, 1, v1
	v_mov_b32_e32 v4, 0xffff
	v_lshlrev_b32_e32 v3, 4, v3
	v_lshlrev_b32_e32 v3, v3, v4
	v_bfe_u32 v7, v1, 1, 2
	v_cmp_eq_u32_e64 s[48:49], 0, v7
	v_cmp_eq_u32_e64 s[50:51], 1, v7
	v_cmp_eq_u32_e64 s[62:63], 2, v7
	v_cmp_eq_u32_e32 vcc, 3, v7
	v_cndmask_b32_e64 v4, 0, v3, s[48:49]
	v_cndmask_b32_e64 v5, 0, v3, s[50:51]
	v_cndmask_b32_e64 v6, 0, v3, s[62:63]
	v_cndmask_b32_e32 v7, 0, v3, vcc
	v_and_b32_e32 v3, 7, v1
	v_lshlrev_b32_e32 v11, 5, v3
	v_and_or_b32 v11, v1, 16, v11
	v_lshrrev_b32_e32 v3, 5, v1
	v_lshl_or_b32 v11, v3, 3, v11
	v_mov_b32_e32 v10, 1
	s_cmp_eq_u32 s6, 1
	s_cbranch_scc1 .Lfd_bmain
	v_mov_b32_e32 v12, 0
	v_mov_b32_e32 v13, 0
	v_mov_b32_e32 v14, 0
	v_mov_b32_e32 v15, 0
	s_and_b32 s47, s6, 1
	s_lshl_b32 s47, s47, 14
	v_lshl_add_u32 v3, v1, 4, s47
	ds_write_b128 v3, v[12:15] offset:8192
	ds_write_b128 v3, v[12:15] offset:9216
	ds_write_b128 v3, v[12:15] offset:10240
	ds_write_b128 v3, v[12:15] offset:11264
	ds_write_b128 v3, v[12:15] offset:12288
	ds_write_b128 v3, v[12:15] offset:13312
	ds_write_b128 v3, v[12:15] offset:14336
	ds_write_b128 v3, v[12:15] offset:15360
	ds_write_b128 v3, v[12:15] offset:16384
	ds_write_b128 v3, v[12:15] offset:17408
	ds_write_b128 v3, v[12:15] offset:18432
	ds_write_b128 v3, v[12:15] offset:19456
	ds_write_b128 v3, v[12:15] offset:20480
	ds_write_b128 v3, v[12:15] offset:21504
	ds_write_b128 v3, v[12:15] offset:22528
	ds_write_b128 v3, v[12:15] offset:23552
	s_waitcnt lgkmcnt(0)
	s_mov_b64 exec, 1
	ds_add_u32 v9, v10 offset:0
	s_mov_b64 exec, -1
	s_branch .Lfd_bmain
.Lfd_ln:
	; ======== layer norm (wave 0) and tile GEMV (waves 0 and 8).
	; GEMV by MFMA on row-major weights: operand A holds x[row] in slot (m mod 8) of row m, so D[m][n] picks column (m mod 8) of lane n's 16 bytes
	v_and_b32_e32 v3, 1, v1
	v_mov_b32_e32 v4, 0xffff
	v_lshlrev_b32_e32 v3, 4, v3
	v_lshlrev_b32_e32 v3, v3, v4
	v_bfe_u32 v7, v1, 1, 2
	v_cmp_eq_u32_e64 s[48:49], 0, v7
	v_cmp_eq_u32_e64 s[50:51], 1, v7
	v_cmp_eq_u32_e64 s[62:63], 2, v7
	v_cmp_eq_u32_e32 vcc, 3, v7
	v_cndmask_b32_e64 v4, 0, v3, s[48:49]
	v_cndmask_b32_e64 v5, 0, v3, s[50:51]
	v_cndmask_b32_e64 v6, 0, v3, s[62:63]
	v_cndmask_b32_e32 v7, 0, v3, vcc
	v_and_b32_e32 v3, 7, v1
	v_lshlrev_b32_e32 v11, 5, v3
	v_and_or_b32 v11, v1, 16, v11
	v_lshrrev_b32_e32 v3, 5, v1
	v_lshl_or_b32 v11, v3, 3, v11
	v_mov_b32_e32 v10, 1
	s_cmp_eq_u32 s6, 0
	s_cbranch_scc0 .Lfd_p1
	s_movk_i32 s52, 0x600
	s_and_b32 s54, s4, 3
	s_mul_i32 s54, s54, 448
	s_add_u32 s55, s54, 224
	s_lshl_b32 s56, s3, 13
	s_and_b32 s47, s4, 1
	s_lshl_b32 s47, s47, 12
	s_add_u32 s56, s56, s47
	s_and_b32 s47, s4, 2
	s_lshl_b32 s47, s47, 10
	s_add_u32 s56, s56, s47
	s_lshr_b32 s47, s4, 2
	s_lshl_b32 s47, s47, 8
	s_add_u32 s56, s56, s47
	s_add_u32 s56, s56, 0x1000
	s_mov_b32 s62, 0x00ff00ff
	s_mov_b32 s63, s62
	s_branch .Lfd_p2
.Lfd_p1:
	s_movk_i32 s52, 0x600
	s_mul_i32 s54, s3, 224
	s_mov_b32 s55, s54
	s_lshl_b32 s56, s4, 12
	s_lshl_b32 s47, s3, 9
	s_add_u32 s56, s56, s47
	s_add_u32 s56, s56, 0x11000
	s_mov_b64 s[62:63], -1
.Lfd_p2:
	s_add_u32 s54, s54, s52
	s_add_u32 s55, s55, s52
	v_lshl_add_u32 v19, v1, 5, s52
	v_lshl_add_u32 v17, v18, 2, s54
	v_lshl_add_u32 v18, v18, 2, s55
	v_and_b32_e32 v3, 8, v1
	v_lshl_or_b32 v2, v3, 5, v11
	v_cmp_gt_u32_e64 s[48:49], 56, v1
	v_mov_b32_e32 v0, 0x3f
	s_cmp_eq_u32 s6, 0
	s_cbranch_scc1 .Lfd_ln0
	s_mov_b32 s47, 0x400000
.Lfd_w1:
	ds_read_b32 v3, v9 offset:32
	s_waitcnt lgkmcnt(0)
	v_cmp_gt_u32_e32 vcc, 1, v3
	s_cbranch_vccz .Lfd_w2
	s_sleep 1
	s_sub_u32 s47, s47, 1
	s_cbranch_scc0 .Lfd_w1
.Lfd_w2:
	ds_read_b32 v3, v9 offset:20
	s_waitcnt lgkmcnt(0)
	v_readfirstlane_b32 s44, v3
	s_branch .Lfd_ln1
.Lfd_ln0:
	s_waitcnt vmcnt(15)
	; layer norm, every step rounded to bf16 where the reference rounds
	v_lshlrev_b32_e32 v106, 16, v12
	v_and_b32_e32 v107, 0xffff0000, v12
	v_lshlrev_b32_e32 v108, 16, v13
	v_and_b32_e32 v109, 0xffff0000, v13
	v_lshlrev_b32_e32 v110, 16, v14
	v_and_b32_e32 v111, 0xffff0000, v14
	v_lshlrev_b32_e32 v112, 16, v15
	v_and_b32_e32 v113, 0xffff0000, v15
	v_pk_add_f32 v[114:115], v[106:107], v[108:109]
	v_pk_add_f32 v[116:117], v[110:111], v[112:113]
	v_pk_add_f32 v[114:115], v[114:115], v[116:117]
	v_add_f32_e32 v118, v114, v115
	s_nop 1
	v_add_f32_dpp v118, v118, v118 quad_perm:[1,0,3,2] row_mask:0xf bank_mask:0xf
	s_nop 1
	v_add_f32_dpp v118, v118, v118 quad_perm:[2,3,0,1] row_mask:0xf bank_mask:0xf
	s_nop 1
	v_add_f32_dpp v118, v118, v118 row_half_mirror row_mask:0xf bank_mask:0xf
	s_nop 1
	v_add_f32_dpp v118, v118, v118 row_ror:8 row_mask:0xf bank_mask:0xf
	v_mov_b32_e32 v114, v118
	s_nop 1
	v_permlane16_swap_b32 v118, v114
	v_add_f32_e32 v118, v118, v114
	v_mov_b32_e32 v114, v118
	s_nop 1
	v_permlane32_swap_b32 v118, v114
	v_add_f32_e32 v118, v118, v114
	v_mul_f32_e32 v118, 0x3b124925, v118
	v_pk_add_f32 v[120:121], v[106:107], v[118:119] op_sel_hi:[1,0] neg_lo:[0,1] neg_hi:[0,1]
	v_pk_add_f32 v[122:123], v[108:109], v[118:119] op_sel_hi:[1,0] neg_lo:[0,1] neg_hi:[0,1]
	v_pk_add_f32 v[124:125], v[110:111], v[118:119] op_sel_hi:[1,0] neg_lo:[0,1] neg_hi:[0,1]
	v_pk_add_f32 v[126:127], v[112:113], v[118:119] op_sel_hi:[1,0] neg_lo:[0,1] neg_hi:[0,1]
	v_pk_mul_f32 v[114:115], v[120:121], v[120:121]
	v_pk_mul_f32 v[116:117], v[122:123], v[122:123]
	v_pk_fma_f32 v[114:115], v[124:125], v[124:125], v[114:115]
	v_pk_fma_f32 v[116:117], v[126:127], v[126:127], v[116:117]
	v_pk_add_f32 v[114:115], v[114:115], v[116:117]
	v_cvt_pk_bf16_f32 v118, v118, v118
	v_and_b32_e32 v118, 0xffff0000, v118
	v_add_f32_e32 v114, v114, v115
	v_cndmask_b32_e64 v114, 0, v114, s[48:49]
	v_pk_add_f32 v[120:121], v[106:107], v[118:119] op_sel_hi:[1,0] neg_lo:[0,1] neg_hi:[0,1]
	v_cvt_pk_bf16_f32 v12, v120, v121
	v_add_f32_dpp v114, v114, v114 quad_perm:[1,0,3,2] row_mask:0xf bank_mask:0xf
	v_pk_add_f32 v[122:123], v[108:109], v[118:119] op_sel_hi:[1,0] neg_lo:[0,1] neg_hi:[0,1]
	v_cvt_pk_bf16_f32 v13, v122, v123
	v_add_f32_dpp v114, v114, v114 quad_perm:[2,3,0,1] row_mask:0xf bank_mask:0xf
	v_pk_add_f32 v[124:125], v[110:111], v[118:119] op_sel_hi:[1,0] neg_lo:[0,1] neg_hi:[0,1]
	v_cvt_pk_bf16_f32 v14, v124, v125
	v_add_f32_dpp v114, v114, v114 row_half_mirror row_mask:0xf bank_mask:0xf
	v_pk_add_f32 v[126:127], v[112:113], v[118:119] op_sel_hi:[1,0] neg_lo:[0,1] neg_hi:[0,1]
)ASM"
R"ASM(
	v_cvt_pk_bf16_f32 v15, v126, v127
	v_add_f32_dpp v114, v114, v114 row_ror:8 row_mask:0xf bank_mask:0xf
	v_mov_b32_e32 v116, v114
	s_nop 1
	v_permlane16_swap_b32 v114, v116
	v_add_f32_e32 v114, v114, v116
	v_mov_b32_e32 v116, v114
	s_nop 1
	v_permlane32_swap_b32 v114, v116
	v_add_f32_e32 v114, v114, v116
	v_mul_f32_e32 v114, 0x3b124925, v114
	v_cvt_pk_bf16_f32 v114, v114, v114
	v_and_b32_e32 v114, 0xffff0000, v114
	v_add_f32_e32 v114, 0x3727c5ac, v114
	v_cvt_pk_bf16_f32 v114, v114, v114
	v_and_b32_e32 v114, 0xffff0000, v114
	v_sqrt_f32_e32 v114, v114
	v_lshlrev_b32_e32 v120, 16, v12
	v_and_b32_e32 v121, 0xffff0000, v12
	v_lshlrev_b32_e32 v122, 16, v13
	v_and_b32_e32 v123, 0xffff0000, v13
	v_cvt_pk_bf16_f32 v114, v114, v114
	v_and_b32_e32 v114, 0xffff0000, v114
	v_rcp_f32_e32 v114, v114
	v_lshlrev_b32_e32 v124, 16, v14
	v_and_b32_e32 v125, 0xffff0000, v14
	v_lshlrev_b32_e32 v126, 16, v15
	v_and_b32_e32 v127, 0xffff0000, v15
	v_pk_mul_f32 v[120:121], v[120:121], v[114:115] op_sel_hi:[1,0]
	v_pk_mul_f32 v[122:123], v[122:123], v[114:115] op_sel_hi:[1,0]
	v_pk_mul_f32 v[124:125], v[124:125], v[114:115] op_sel_hi:[1,0]
	v_pk_mul_f32 v[126:127], v[126:127], v[114:115] op_sel_hi:[1,0]
	v_cvt_pk_bf16_f32 v106, v120, v120
	v_cvt_pk_bf16_f32 v107, v121, v121
	v_cvt_pk_bf16_f32 v108, v122, v122
	v_cvt_pk_bf16_f32 v109, v123, v123
	ds_write_b128 v19, v[106:109]
	v_cvt_pk_bf16_f32 v110, v124, v124
	v_cvt_pk_bf16_f32 v111, v125, v125
	v_cvt_pk_bf16_f32 v112, v126, v126
	v_cvt_pk_bf16_f32 v113, v127, v127
	ds_write_b128 v19, v[110:113] offset:16
	; tag of this call = epoch word + 1; xn and the tag go to wave 8 through LDS
	s_waitcnt vmcnt(14)
	v_add_u32_e32 v3, 1, v16
	ds_write_b32 v9, v3 offset:20
	v_readfirstlane_b32 s44, v3
	s_waitcnt lgkmcnt(0)
	s_mov_b64 exec, 1
	ds_add_u32 v9, v10 offset:32
	s_mov_b64 exec, -1
.Lfd_ln1:
	ds_read2_b32 v[76:77], v17 offset0:0 offset1:8
	ds_read2_b32 v[78:79], v17 offset0:16 offset1:24
	ds_read2_b32 v[80:81], v17 offset0:32 offset1:40
	ds_read_b32 v82, v17 offset:192
	ds_read2_b32 v[84:85], v18 offset0:0 offset1:8
	ds_read2_b32 v[86:87], v18 offset0:16 offset1:24
	ds_read2_b32 v[88:89], v18 offset0:32 offset1:40
	ds_read_b32 v90, v18 offset:192
	s_and_b32 s45, s44, 0x3f
	s_bfe_u32 s46, s44, 0x50006
	ds_read_b64 v[122:123], v9 offset:48
	s_waitcnt lgkmcnt(0)
	; vote for the near half, sent in one tag bit of the q partials (no measurement yet: vote by parity)
	v_readfirstlane_b32 s47, v122
	v_readfirstlane_b32 s52, v123
	s_cmp_lt_u32 s47, s52
	s_cselect_b32 s53, 0x20, 0
	s_min_u32 s47, s47, s52
	s_and_b32 s52, s4, 1
	s_lshl_b32 s52, s52, 5
	s_cmp_eq_u32 s47, 0
	s_cselect_b32 s53, s52, s53
	s_or_b32 s46, s46, s53
	v_and_b32_e32 v92, v4, v76
	v_and_b32_e32 v93, v5, v76
	v_and_b32_e32 v94, v6, v76
	v_and_b32_e32 v95, v7, v76
	s_waitcnt vmcnt(13)
	s_nop 0
	v_mfma_f32_16x16x32_bf16 v[100:103], v[92:95], v[20:23], 0
	v_and_b32_e32 v96, v4, v77
	v_and_b32_e32 v97, v5, v77
	v_and_b32_e32 v98, v6, v77
	v_and_b32_e32 v99, v7, v77
	s_waitcnt vmcnt(12)
	s_nop 0
	v_mfma_f32_16x16x32_bf16 v[100:103], v[96:99], v[24:27], v[100:103]
	v_and_b32_e32 v92, v4, v78
	v_and_b32_e32 v93, v5, v78
	v_and_b32_e32 v94, v6, v78
	v_and_b32_e32 v95, v7, v78
	s_waitcnt vmcnt(11)
	s_nop 0
	v_mfma_f32_16x16x32_bf16 v[100:103], v[92:95], v[28:31], v[100:103]
	v_and_b32_e32 v96, v4, v79
	v_and_b32_e32 v97, v5, v79
	v_and_b32_e32 v98, v6, v79
	v_and_b32_e32 v99, v7, v79
	s_waitcnt vmcnt(10)
	s_nop 0
	v_mfma_f32_16x16x32_bf16 v[100:103], v[96:99], v[32:35], v[100:103]
	v_and_b32_e32 v92, v4, v80
	v_and_b32_e32 v93, v5, v80
	v_and_b32_e32 v94, v6, v80
	v_and_b32_e32 v95, v7, v80
	s_waitcnt vmcnt(9)
	s_nop 0
	v_mfma_f32_16x16x32_bf16 v[100:103], v[92:95], v[36:39], v[100:103]
	v_and_b32_e32 v96, v4, v81
	v_and_b32_e32 v97, v5, v81
	v_and_b32_e32 v98, v6, v81
	v_and_b32_e32 v99, v7, v81
	s_waitcnt vmcnt(8)
	s_nop 0
	v_mfma_f32_16x16x32_bf16 v[100:103], v[96:99], v[40:43], v[100:103]
	v_and_b32_e32 v92, v4, v82
	v_and_b32_e32 v93, v5, v82
	v_and_b32_e32 v94, v6, v82
	v_and_b32_e32 v95, v7, v82
	s_waitcnt vmcnt(7)
	s_nop 0
	v_mfma_f32_16x16x32_bf16 v[100:103], v[92:95], v[44:47], v[100:103]
	v_and_b32_e32 v96, v4, v84
	v_and_b32_e32 v97, v5, v84
	v_and_b32_e32 v98, v6, v84
	v_and_b32_e32 v99, v7, v84
	s_waitcnt vmcnt(6)
	s_nop 0
	v_mfma_f32_16x16x32_bf16 v[104:107], v[96:99], v[48:51], 0
	v_and_b32_e32 v92, v4, v85
	v_and_b32_e32 v93, v5, v85
	v_and_b32_e32 v94, v6, v85
	v_and_b32_e32 v95, v7, v85
	s_waitcnt vmcnt(5)
	s_nop 0
	v_mfma_f32_16x16x32_bf16 v[104:107], v[92:95], v[52:55], v[104:107]
	v_and_b32_e32 v96, v4, v86
	v_and_b32_e32 v97, v5, v86
	v_and_b32_e32 v98, v6, v86
	v_and_b32_e32 v99, v7, v86
	s_waitcnt vmcnt(4)
	s_nop 0
	v_mfma_f32_16x16x32_bf16 v[104:107], v[96:99], v[56:59], v[104:107]
	v_and_b32_e32 v92, v4, v87
	v_and_b32_e32 v93, v5, v87
	v_and_b32_e32 v94, v6, v87
	v_and_b32_e32 v95, v7, v87
	s_waitcnt vmcnt(3)
	s_nop 0
	v_mfma_f32_16x16x32_bf16 v[104:107], v[92:95], v[60:63], v[104:107]
	v_and_b32_e32 v96, v4, v88
	v_and_b32_e32 v97, v5, v88
	v_and_b32_e32 v98, v6, v88
	v_and_b32_e32 v99, v7, v88
	s_waitcnt vmcnt(2)
	s_nop 0
	v_mfma_f32_16x16x32_bf16 v[104:107], v[96:99], v[64:67], v[104:107]
	v_and_b32_e32 v92, v4, v89
	v_and_b32_e32 v93, v5, v89
	v_and_b32_e32 v94, v6, v89
	v_and_b32_e32 v95, v7, v89
	s_waitcnt vmcnt(1)
	s_nop 0
	v_mfma_f32_16x16x32_bf16 v[104:107], v[92:95], v[68:71], v[104:107]
	v_and_b32_e32 v96, v4, v90
	v_and_b32_e32 v97, v5, v90
	v_and_b32_e32 v98, v6, v90
	v_and_b32_e32 v99, v7, v90
	s_waitcnt vmcnt(0)
	s_nop 0
	v_mfma_f32_16x16x32_bf16 v[104:107], v[96:99], v[72:75], v[104:107]
	s_cmp_lg_u32 s6, 0
	s_nop 6
	s_cbranch_scc1 .Lfd_nosum
	v_pk_add_f32 v[100:101], v[100:101], v[104:105]
	v_pk_add_f32 v[102:103], v[102:103], v[106:107]
	s_nop 1
.Lfd_nosum:
	; fold the two row halves; publish pairs of 26-bit floats with the tag in the low bits of both words
	v_permlane32_swap_b32 v100, v102
	v_permlane32_swap_b32 v101, v103
	v_permlane32_swap_b32 v104, v106
	v_add_f32_dpp v12, v102, v100 row_ror:8 row_mask:0xf bank_mask:0x3
	v_permlane32_swap_b32 v105, v107
	v_add_f32_dpp v13, v103, v101 row_ror:8 row_mask:0xf bank_mask:0x3
	v_add_f32_dpp v12, v104, v106 row_ror:8 row_mask:0xf bank_mask:0xc
	v_add_u32_e32 v12, 0x20, v12
	v_add_f32_dpp v13, v105, v107 row_ror:8 row_mask:0xf bank_mask:0xc
	v_add_u32_e32 v13, 0x20, v13
	v_bfi_b32 v12, v0, s45, v12
	v_bfi_b32 v13, v0, s46, v13
	s_mov_b64 exec, s[62:63]
	buffer_store_dwordx2 v[12:13], v2, s[28:31], s56 offen sc1
	s_mov_b64 exec, -1
	s_cmp_lg_u32 s6, 0
	s_cbranch_scc1 .Lfd_f2
	s_waitcnt lgkmcnt(0)
	s_mov_b64 exec, 1
	ds_add_u32 v9, v10 offset:24
	s_mov_b64 exec, -1
.Lfd_bmain:
	; ======== waves 0..3: V (64 dims x 128 keys each, behind K in the queue), P.V, publish this split's partial o
	s_setprio 0
	s_mul_i32 s7, s4, 3
	s_mul_i32 s52, s3, 7
	s_add_u32 s7, s7, s52
	s_lshl_b32 s52, s7, 12
	s_lshl_b32 s54, s3, 8
	s_add_u32 s54, s54, s4
	s_addk_i32 s54, 0xe0
	s_lshl_b32 s54, s54, 16
	s_add_u32 s24, s16, s54
	s_addc_u32 s25, s17, 0
	s_mov_b32 s26, 0x10000
	s_mov_b32 s27, s43
	s_lshl_b32 s47, s5, 7
	v_lshrrev_b32_e32 v17, 3, v1
	v_and_b32_e32 v3, 7, v1
	v_lshl_add_u32 v3, v3, 4, s47
	v_lshl_or_b32 v17, v17, 9, v3
	v_lshlrev_b32_e32 v18, 4, v1
	v_and_b32_e32 v3, 8, v1
	v_lshl_or_b32 v19, v3, 7, v11
	s_lshl_b32 s56, s3, 5
	s_add_u32 s56, s56, s4
	s_lshl_b32 s56, s56, 11
	s_lshl_b32 s47, s5, 8
	v_cmp_eq_u32_e64 s[48:49], 0, v3
	s_and_b32 s55, s56, 0xffffe000
	s_add_u32 s56, s56, s55
	s_add_u32 s56, s56, s47
	s_add_u32 s56, s56, 0x40000
	s_mov_b32 s47, 0x400000
.Lfd_w3:
	ds_read_b32 v3, v9 offset:28
	s_waitcnt lgkmcnt(0)
	v_cmp_gt_u32_e32 vcc, 4, v3
	s_cbranch_vccz .Lfd_w4
	s_sleep 1
	s_sub_u32 s47, s47, 1
	s_cbranch_scc0 .Lfd_w3
.Lfd_w4:
	s_and_b32 s55, s52, 0xf000
	buffer_load_dwordx4 v[20:23], v17, s[24:27], s55 offen nt
	s_addk_i32 s52, 0x1000
	s_and_b32 s55, s52, 0xf000
	buffer_load_dwordx4 v[24:27], v17, s[24:27], s55 offen nt
	s_addk_i32 s52, 0x1000
	s_and_b32 s55, s52, 0xf000
	buffer_load_dwordx4 v[28:31], v17, s[24:27], s55 offen nt
	s_addk_i32 s52, 0x1000
	s_and_b32 s55, s52, 0xf000
	buffer_load_dwordx4 v[32:35], v17, s[24:27], s55 offen nt
	s_addk_i32 s52, 0x1000
	s_and_b32 s55, s52, 0xf000
	buffer_load_dwordx4 v[36:39], v17, s[24:27], s55 offen nt
	s_addk_i32 s52, 0x1000
	s_and_b32 s55, s52, 0xf000
	buffer_load_dwordx4 v[40:43], v17, s[24:27], s55 offen nt
	s_addk_i32 s52, 0x1000
	s_and_b32 s55, s52, 0xf000
	buffer_load_dwordx4 v[44:47], v17, s[24:27], s55 offen nt
	s_addk_i32 s52, 0x1000
	s_and_b32 s55, s52, 0xf000
	buffer_load_dwordx4 v[48:51], v17, s[24:27], s55 offen nt
	s_addk_i32 s52, 0x1000
	s_and_b32 s55, s52, 0xf000
	buffer_load_dwordx4 v[52:55], v17, s[24:27], s55 offen nt
	s_addk_i32 s52, 0x1000
	s_and_b32 s55, s52, 0xf000
	buffer_load_dwordx4 v[56:59], v17, s[24:27], s55 offen nt
	s_addk_i32 s52, 0x1000
	s_and_b32 s55, s52, 0xf000
	buffer_load_dwordx4 v[60:63], v17, s[24:27], s55 offen nt
	s_addk_i32 s52, 0x1000
	s_and_b32 s55, s52, 0xf000
	buffer_load_dwordx4 v[64:67], v17, s[24:27], s55 offen nt
	s_addk_i32 s52, 0x1000
	s_and_b32 s55, s52, 0xf000
	buffer_load_dwordx4 v[68:71], v17, s[24:27], s55 offen nt
	s_addk_i32 s52, 0x1000
	s_and_b32 s55, s52, 0xf000
	buffer_load_dwordx4 v[72:75], v17, s[24:27], s55 offen nt
	s_addk_i32 s52, 0x1000
	s_and_b32 s55, s52, 0xf000
	buffer_load_dwordx4 v[76:79], v17, s[24:27], s55 offen nt
	s_addk_i32 s52, 0x1000
	s_and_b32 s55, s52, 0xf000
	buffer_load_dwordx4 v[80:83], v17, s[24:27], s55 offen nt
	s_waitcnt lgkmcnt(0)
	s_mov_b64 exec, 1
	ds_add_u32 v9, v10 offset:12
	s_mov_b64 exec, -1
	; wait for the probabilities; factors exp2(m_wave - m_split) per group of 32 keys; operands come ready-made from the LDS table
	s_mov_b32 s47, 0x400000
.Lfd_w5:
	ds_read_b32 v3, v9 offset:4
	s_waitcnt lgkmcnt(0)
	v_cmp_gt_u32_e32 vcc, 4, v3
	s_cbranch_vccz .Lfd_w6
	s_sleep 1
	s_sub_u32 s47, s47, 1
	s_cbranch_scc0 .Lfd_w5
.Lfd_w6:
	s_waitcnt vmcnt(16)
	ds_read_b32 v3, v9 offset:20
	ds_read_b32 v2, v9 offset:44
	v_and_b32_e32 v12, 8, v1
	v_lshlrev_b32_e32 v12, 2, v12
	ds_read_b128 v[156:159], v12 offset:64
	ds_read_b128 v[160:163], v12 offset:80
	ds_read_b128 v[84:87], v18 offset:8192
	ds_read_b128 v[88:91], v18 offset:24576
	ds_read_b128 v[92:95], v18 offset:9216
	ds_read_b128 v[96:99], v18 offset:25600
	ds_read_b128 v[100:103], v18 offset:10240
	ds_read_b128 v[104:107], v18 offset:26624
	ds_read_b128 v[108:111], v18 offset:11264
	ds_read_b128 v[112:115], v18 offset:27648
	s_waitcnt lgkmcnt(8)
	v_readfirstlane_b32 s44, v3
	v_readfirstlane_b32 s34, v2
	s_cmp_ge_u32 s34, 264
	s_cselect_b32 s34, 0, 0x2000
	s_add_u32 s56, s56, s34
	v_max_f32_e32 v12, v156, v158
	v_max3_f32 v12, v160, v162, v12
	v_sub_f32_e32 v4, v156, v12
	v_sub_f32_e32 v5, v158, v12
	v_sub_f32_e32 v6, v160, v12
	v_sub_f32_e32 v7, v162, v12
	v_exp_f32_e32 v4, v4
	v_exp_f32_e32 v5, v5
	v_exp_f32_e32 v6, v6
	v_exp_f32_e32 v7, v7
	v_mov_b32_e32 v15, s44
	s_waitcnt lgkmcnt(0)
	ds_read_b128 v[116:119], v18 offset:12288
	ds_read_b128 v[120:123], v18 offset:28672
	ds_read_b128 v[124:127], v18 offset:13312
	ds_read_b128 v[128:131], v18 offset:29696
	ds_read_b128 v[132:135], v18 offset:14336
	ds_read_b128 v[136:139], v18 offset:30720
	ds_read_b128 v[140:143], v18 offset:15360
	ds_read_b128 v[144:147], v18 offset:31744
	s_waitcnt vmcnt(15)
	v_mfma_f32_16x16x32_bf16 v[148:151], v[84:87], v[20:23], 0
	v_mfma_f32_16x16x32_bf16 v[152:155], v[88:91], v[20:23], 0
	s_waitcnt vmcnt(14)
	v_mfma_f32_16x16x32_bf16 v[148:151], v[92:95], v[24:27], v[148:151]
	v_mfma_f32_16x16x32_bf16 v[152:155], v[96:99], v[24:27], v[152:155]
	s_waitcnt vmcnt(13)
	v_mfma_f32_16x16x32_bf16 v[148:151], v[100:103], v[28:31], v[148:151]
	v_mfma_f32_16x16x32_bf16 v[152:155], v[104:107], v[28:31], v[152:155]
	s_waitcnt vmcnt(12)
	v_mfma_f32_16x16x32_bf16 v[148:151], v[108:111], v[32:35], v[148:151]
	v_mfma_f32_16x16x32_bf16 v[152:155], v[112:115], v[32:35], v[152:155]
	ds_read_b128 v[84:87], v18 offset:16384
	ds_read_b128 v[88:91], v18 offset:32768
	ds_read_b128 v[92:95], v18 offset:17408
)ASM"
R"ASM(
	ds_read_b128 v[96:99], v18 offset:33792
	ds_read_b128 v[100:103], v18 offset:18432
	ds_read_b128 v[104:107], v18 offset:34816
	ds_read_b128 v[108:111], v18 offset:19456
	ds_read_b128 v[112:115], v18 offset:35840
	s_waitcnt lgkmcnt(8)
	s_waitcnt vmcnt(11)
	v_mfma_f32_16x16x32_bf16 v[156:159], v[116:119], v[36:39], 0
	v_mfma_f32_16x16x32_bf16 v[160:163], v[120:123], v[36:39], 0
	s_waitcnt vmcnt(10)
	v_mfma_f32_16x16x32_bf16 v[156:159], v[124:127], v[40:43], v[156:159]
	v_mfma_f32_16x16x32_bf16 v[160:163], v[128:131], v[40:43], v[160:163]
	s_waitcnt vmcnt(9)
	v_mfma_f32_16x16x32_bf16 v[156:159], v[132:135], v[44:47], v[156:159]
	v_mfma_f32_16x16x32_bf16 v[160:163], v[136:139], v[44:47], v[160:163]
	s_nop 0
	v_permlane32_swap_b32 v148, v150
	v_permlane32_swap_b32 v149, v151
	v_permlane32_swap_b32 v152, v154
	v_add_f32_dpp v16, v150, v148 row_ror:8 row_mask:0xf bank_mask:0x3
	v_permlane32_swap_b32 v153, v155
	v_add_f32_dpp v17, v151, v149 row_ror:8 row_mask:0xf bank_mask:0x3
	v_add_f32_dpp v16, v152, v154 row_ror:8 row_mask:0xf bank_mask:0xc
	v_mul_f32_e32 v12, v4, v16
	v_add_f32_dpp v17, v153, v155 row_ror:8 row_mask:0xf bank_mask:0xc
	v_mul_f32_e32 v13, v4, v17
	s_waitcnt vmcnt(8)
	v_mfma_f32_16x16x32_bf16 v[156:159], v[140:143], v[48:51], v[156:159]
	v_mfma_f32_16x16x32_bf16 v[160:163], v[144:147], v[48:51], v[160:163]
	ds_read_b128 v[116:119], v18 offset:20480
	ds_read_b128 v[120:123], v18 offset:36864
	ds_read_b128 v[124:127], v18 offset:21504
	ds_read_b128 v[128:131], v18 offset:37888
	ds_read_b128 v[132:135], v18 offset:22528
	ds_read_b128 v[136:139], v18 offset:38912
	ds_read_b128 v[140:143], v18 offset:23552
	ds_read_b128 v[144:147], v18 offset:39936
	s_waitcnt lgkmcnt(8)
	s_waitcnt vmcnt(7)
	v_mfma_f32_16x16x32_bf16 v[148:151], v[84:87], v[52:55], 0
	v_mfma_f32_16x16x32_bf16 v[152:155], v[88:91], v[52:55], 0
	s_waitcnt vmcnt(6)
	v_mfma_f32_16x16x32_bf16 v[148:151], v[92:95], v[56:59], v[148:151]
	v_mfma_f32_16x16x32_bf16 v[152:155], v[96:99], v[56:59], v[152:155]
	s_waitcnt vmcnt(5)
	v_mfma_f32_16x16x32_bf16 v[148:151], v[100:103], v[60:63], v[148:151]
	v_mfma_f32_16x16x32_bf16 v[152:155], v[104:107], v[60:63], v[152:155]
	s_nop 0
	v_permlane32_swap_b32 v156, v158
	v_permlane32_swap_b32 v157, v159
	v_permlane32_swap_b32 v160, v162
	v_add_f32_dpp v16, v158, v156 row_ror:8 row_mask:0xf bank_mask:0x3
	v_permlane32_swap_b32 v161, v163
	v_add_f32_dpp v17, v159, v157 row_ror:8 row_mask:0xf bank_mask:0x3
	v_add_f32_dpp v16, v160, v162 row_ror:8 row_mask:0xf bank_mask:0xc
	v_fmac_f32_e32 v12, v5, v16
	v_add_f32_dpp v17, v161, v163 row_ror:8 row_mask:0xf bank_mask:0xc
	v_fmac_f32_e32 v13, v5, v17
	s_waitcnt vmcnt(4)
	v_mfma_f32_16x16x32_bf16 v[148:151], v[108:111], v[64:67], v[148:151]
	v_mfma_f32_16x16x32_bf16 v[152:155], v[112:115], v[64:67], v[152:155]
	s_waitcnt lgkmcnt(0)
	s_waitcnt vmcnt(3)
	v_mfma_f32_16x16x32_bf16 v[156:159], v[116:119], v[68:71], 0
	v_mfma_f32_16x16x32_bf16 v[160:163], v[120:123], v[68:71], 0
	s_waitcnt vmcnt(2)
	v_mfma_f32_16x16x32_bf16 v[156:159], v[124:127], v[72:75], v[156:159]
	v_mfma_f32_16x16x32_bf16 v[160:163], v[128:131], v[72:75], v[160:163]
	s_waitcnt vmcnt(1)
	v_mfma_f32_16x16x32_bf16 v[156:159], v[132:135], v[76:79], v[156:159]
	v_mfma_f32_16x16x32_bf16 v[160:163], v[136:139], v[76:79], v[160:163]
	s_nop 0
	v_permlane32_swap_b32 v148, v150
	v_permlane32_swap_b32 v149, v151
	v_permlane32_swap_b32 v152, v154
	v_add_f32_dpp v16, v150, v148 row_ror:8 row_mask:0xf bank_mask:0x3
	v_permlane32_swap_b32 v153, v155
	v_add_f32_dpp v17, v151, v149 row_ror:8 row_mask:0xf bank_mask:0x3
	v_add_f32_dpp v16, v152, v154 row_ror:8 row_mask:0xf bank_mask:0xc
	v_fmac_f32_e32 v12, v6, v16
	v_add_f32_dpp v17, v153, v155 row_ror:8 row_mask:0xf bank_mask:0xc
	v_fmac_f32_e32 v13, v6, v17
	s_waitcnt vmcnt(0)
	v_mfma_f32_16x16x32_bf16 v[156:159], v[140:143], v[80:83], v[156:159]
	v_mfma_f32_16x16x32_bf16 v[160:163], v[144:147], v[80:83], v[160:163]
	; last group, bf16 pair + tag per lane into the near half
	s_nop 6
	v_permlane32_swap_b32 v156, v158
	v_permlane32_swap_b32 v157, v159
	v_permlane32_swap_b32 v160, v162
	v_add_f32_dpp v16, v158, v156 row_ror:8 row_mask:0xf bank_mask:0x3
	v_permlane32_swap_b32 v161, v163
	v_add_f32_dpp v17, v159, v157 row_ror:8 row_mask:0xf bank_mask:0x3
	v_add_f32_dpp v16, v160, v162 row_ror:8 row_mask:0xf bank_mask:0xc
	v_fmac_f32_e32 v12, v7, v16
	v_add_f32_dpp v17, v161, v163 row_ror:8 row_mask:0xf bank_mask:0xc
	v_fmac_f32_e32 v13, v7, v17
	v_cvt_pk_bf16_f32 v14, v12, v13
	buffer_store_dwordx2 v[14:15], v19, s[28:31], s56 offen sc1
	s_waitcnt lgkmcnt(0)
	s_mov_b64 exec, 1
	ds_add_u32 v9, v10 offset:8
	s_mov_b64 exec, -1
	s_endpgm
.Lfd_a1:
	; ======== waves 4..7: the rest of K goes out once wave 0's q tile is on its way (keeps that store from queueing behind it)
	s_cmp_lt_u32 s4, 28
	s_cbranch_scc0 .Lfd_a2
	v_mov_b32_e32 v10, 1
	s_mov_b32 s47, 0x400000
.Lfd_w7:
	ds_read_b32 v3, v9 offset:24
	s_waitcnt lgkmcnt(0)
	v_cmp_gt_u32_e32 vcc, 1, v3
	s_cbranch_vccz .Lfd_w8
	s_sleep 1
	s_sub_u32 s47, s47, 1
	s_cbranch_scc0 .Lfd_w7
.Lfd_w8:
.Lfd_a2:
	global_load_dwordx4 v[40:43], v18, s[58:59] offset:128 nt
	global_load_dwordx4 v[44:47], v18, s[58:59] offset:256 nt
	global_load_dwordx4 v[48:51], v18, s[58:59] offset:384 nt
	s_cmp_lt_u32 s4, 28
	s_cbranch_scc1 .Lfd_a3
	s_mov_b32 s47, 0x400000
.Lfd_w9:
	ds_read_b32 v3, v9 offset:24
	s_waitcnt lgkmcnt(0)
	v_cmp_gt_u32_e32 vcc, 1, v3
	s_cbranch_vccz .Lfd_w10
	s_sleep 1
	s_sub_u32 s47, s47, 1
	s_cbranch_scc0 .Lfd_w9
.Lfd_w10:
.Lfd_a3:
	s_addk_i32 s52, 0x1000
	s_and_b32 s55, s52, 0xf000
	v_or_b32_e32 v18, s55, v17
	global_load_dwordx4 v[52:55], v18, s[58:59] offset:0 nt
	global_load_dwordx4 v[56:59], v18, s[58:59] offset:128 nt
	global_load_dwordx4 v[60:63], v18, s[58:59] offset:256 nt
	global_load_dwordx4 v[64:67], v18, s[58:59] offset:384 nt
	s_addk_i32 s52, 0x1000
	s_and_b32 s55, s52, 0xf000
	v_or_b32_e32 v18, s55, v17
	global_load_dwordx4 v[68:71], v18, s[58:59] offset:0 nt
	global_load_dwordx4 v[72:75], v18, s[58:59] offset:128 nt
	global_load_dwordx4 v[76:79], v18, s[58:59] offset:256 nt
	global_load_dwordx4 v[80:83], v18, s[58:59] offset:384 nt
	v_mov_b32_e32 v10, 1
	s_waitcnt lgkmcnt(0)
	s_mov_b64 exec, 1
	ds_add_u32 v9, v10 offset:28
	s_mov_b64 exec, -1
	; s24 / s25 row group and column block of the wo tile; q gather (4 partial sums x 128 columns per wave) and the wo tile
	s_mul_i32 s24, s4, 0x2493
	s_lshr_b32 s24, s24, 16
	s_mul_i32 s25, s24, 7
	s_sub_u32 s25, s4, s25
	s_and_b32 s47, s24, 3
	s_lshl2_add_u32 s47, s3, s47
	s_lshl2_add_u32 s47, s47, s5
	s_mul_i32 s47, s47, 0x7000
	s_lshl_b32 s54, s25, 7
	s_add_u32 s47, s47, s54
	s_add_u32 s36, s18, s47
	s_addc_u32 s37, s19, 0
	s_cmp_lt_u32 s4, 28
	s_cselect_b32 s38, 0x7000, 0
	s_mov_b32 s39, s43
	s_lshl_b32 s54, s3, 13
	s_lshl_b32 s47, s5, 9
	s_add_u32 s54, s54, s47
	s_add_u32 s54, s54, 0x1000
	s_add_u32 s55, s54, 0x1000
	v_lshrrev_b32_e32 v3, 5, v1
	v_and_b32_e32 v17, 31, v1
	v_lshlrev_b32_e32 v16, 4, v17
	v_lshl_or_b32 v16, v3, 11, v16
	v_lshlrev_b32_e32 v17, 3, v17
	v_lshl_or_b32 v17, v3, 2, v17
	v_lshrrev_b32_e32 v18, 3, v1
	v_and_b32_e32 v3, 7, v1
	v_lshlrev_b32_e32 v3, 4, v3
	s_movk_i32 s47, 0xe00
	v_mad_u32_u24 v19, v18, s47, v3
	s_waitcnt vmcnt(11)
	buffer_load_dwordx4 v[84:87], v16, s[28:31], s54 offen sc1
	buffer_load_dwordx4 v[88:91], v16, s[28:31], s55 offen sc1
	buffer_load_dwordx4 v[92:95], v19, s[36:39], 0 offen offset:0 nt
	buffer_load_dwordx4 v[96:99], v19, s[36:39], 0 offen offset:896 nt
	buffer_load_dwordx4 v[100:103], v19, s[36:39], 0 offen offset:1792 nt
	buffer_load_dwordx4 v[104:107], v19, s[36:39], 0 offen offset:2688 nt
	ds_read_b32 v3, v9 offset:20
	s_waitcnt lgkmcnt(0)
	v_readfirstlane_b32 s44, v3
	s_and_b32 s45, s44, 0x3f
	s_bfe_u32 s46, s44, 0x50006
	v_and_b32_e32 v3, 1, v1
	v_mov_b32_e32 v4, 0xffff
	v_lshlrev_b32_e32 v3, 4, v3
	v_lshlrev_b32_e32 v3, v3, v4
	v_bfe_u32 v7, v1, 1, 2
	v_cmp_eq_u32_e64 s[48:49], 0, v7
	v_cmp_eq_u32_e64 s[50:51], 1, v7
	v_cmp_eq_u32_e64 s[62:63], 2, v7
	v_cmp_eq_u32_e32 vcc, 3, v7
	v_cndmask_b32_e64 v4, 0, v3, s[48:49]
	v_cndmask_b32_e64 v5, 0, v3, s[50:51]
	v_cndmask_b32_e64 v6, 0, v3, s[62:63]
	v_cndmask_b32_e32 v7, 0, v3, vcc
	v_and_b32_e32 v3, 7, v1
	v_lshlrev_b32_e32 v11, 5, v3
	v_and_or_b32 v11, v1, 16, v11
	v_lshrrev_b32_e32 v3, 5, v1
	v_lshl_or_b32 v11, v3, 3, v11
	v_mov_b32_e32 v10, 1
	s_lshr_b32 s47, s5, 1
	s_mul_i32 s47, s47, 640
	s_and_b32 s52, s5, 1
	s_lshl_b32 s52, s52, 8
	s_add_u32 s47, s47, s52
	v_add_u32_e32 v17, s47, v17
	v_bfe_u32 v15, v1, 2, 1
	v_and_b32_e32 v3, 8, v1
	v_lshlrev_b32_e32 v12, 3, v3
	v_and_or_b32 v12, v1, 48, v12
	s_movk_i32 s47, 0x280
	v_mad_u32_u24 v12, v15, s47, v12
	v_lshrrev_b32_e32 v3, 1, v3
	v_and_or_b32 v3, v1, 3, v3
	v_mul_u32_u24_e32 v13, 18, v3
	v_lshl_or_b32 v2, v15, 14, v13
	v_and_b32_e32 v3, 16, v1
	v_lshl_add_u32 v2, v3, 5, v2
	v_and_b32_e32 v3, 32, v1
	v_lshl_add_u32 v2, v3, 6, v2
	s_lshl_b32 s47, s5, 12
	v_add_u32_e32 v13, s47, v2
	s_lshl_b32 s47, s5, 3
	v_lshl_add_u32 v14, v15, 5, s47
	v_mov_b32_e32 v0, 0xffffffc0
	s_mov_b32 s47, 0x10000
	s_waitcnt vmcnt(4)
.Lfd_g11:
	v_bitop3_b32 v149, v85, v87, s46 bitop3:0x7e
	v_bitop3_b32 v148, v84, v86, s45 bitop3:0x7e
	v_lshl_or_b32 v2, v149, 1, v148
	v_bitop3_b32 v149, v89, v91, s46 bitop3:0x7e
	v_bitop3_b32 v148, v88, v90, s45 bitop3:0x7e
	v_lshl_or_b32 v148, v149, 1, v148
	v_or_b32_e32 v2, v2, v148
	v_and_b32_e32 v2, 0x3f, v2
	v_cmp_ne_u32_e32 vcc, 0, v2
	s_cbranch_vccz .Lfd_g12
	s_mov_b64 exec, vcc
	buffer_load_dwordx4 v[84:87], v16, s[28:31], s54 offen sc1
	buffer_load_dwordx4 v[88:91], v16, s[28:31], s55 offen sc1
	s_mov_b64 exec, -1
	s_sub_u32 s47, s47, 1
	s_waitcnt vmcnt(0)
	s_cbranch_scc0 .Lfd_g11
.Lfd_g12:
	; count the votes (all 32 workgroups of a head see the same 32 votes), sum the partials, q as bf16 to LDS
	v_and_b32_e32 v2, 0x20, v85
	v_and_b32_e32 v3, 0x20, v89
	v_cmp_ne_u32_e32 vcc, 0, v2
	s_bcnt1_i32_b64 s47, vcc
	v_cmp_ne_u32_e32 vcc, 0, v3
	s_bcnt1_i32_b64 s52, vcc
	s_add_u32 s47, s47, s52
	v_mov_b32_e32 v2, s47
	v_and_b32_e32 v148, v0, v84
	v_and_b32_e32 v149, v0, v85
	v_and_b32_e32 v150, v0, v86
	v_and_b32_e32 v151, v0, v87
	v_and_b32_e32 v88, v0, v88
	v_and_b32_e32 v89, v0, v89
	v_and_b32_e32 v90, v0, v90
	v_and_b32_e32 v91, v0, v91
	v_pk_add_f32 v[148:149], v[148:149], v[88:89]
	v_pk_add_f32 v[150:151], v[150:151], v[90:91]
	s_nop 1
	v_permlane32_swap_b32 v148, v150
	v_permlane32_swap_b32 v149, v151
	v_pk_add_f32 v[148:149], v[148:149], v[150:151]
	v_cvt_pk_bf16_f32 v148, v148, v149
	ds_write_b32 v17, v148 offset:128
	s_waitcnt lgkmcnt(0)
	s_mov_b64 exec, 1
	ds_add_u32 v9, v2 offset:44
	ds_add_u32 v9, v10 offset:0
	s_mov_b64 exec, -1
	s_mov_b32 s47, 0x400000
.Lfd_w13:
	ds_read_b32 v3, v9 offset:0
	s_waitcnt lgkmcnt(0)
	v_cmp_gt_u32_e32 vcc, 6, v3
	s_cbranch_vccz .Lfd_w14
	s_sub_u32 s47, s47, 1
	s_cbranch_scc0 .Lfd_w13
.Lfd_w14:
	; scores: D rows = 8 keys x 2 halves of a 64-dim block, columns = 2 heads x 2 halves; matching halves are added below
	ds_read_b128 v[108:111], v12 offset:128
	ds_read_b128 v[112:115], v12 offset:256
	ds_read_b128 v[116:119], v12 offset:384
	ds_read_b128 v[120:123], v12 offset:512
	s_waitcnt lgkmcnt(3)
	v_mfma_f32_16x16x32_bf16 v[124:127], v[20:23], v[108:111], 0
	v_mfma_f32_16x16x32_bf16 v[132:135], v[52:55], v[108:111], 0
	v_mfma_f32_16x16x32_bf16 v[128:131], v[36:39], v[108:111], 0
	v_mfma_f32_16x16x32_bf16 v[136:139], v[68:71], v[108:111], 0
	s_waitcnt lgkmcnt(2)
	v_mfma_f32_16x16x32_bf16 v[124:127], v[24:27], v[112:115], v[124:127]
	v_mfma_f32_16x16x32_bf16 v[132:135], v[56:59], v[112:115], v[132:135]
	v_mfma_f32_16x16x32_bf16 v[128:131], v[40:43], v[112:115], v[128:131]
	v_mfma_f32_16x16x32_bf16 v[136:139], v[72:75], v[112:115], v[136:139]
	s_waitcnt lgkmcnt(1)
	v_mfma_f32_16x16x32_bf16 v[124:127], v[28:31], v[116:119], v[124:127]
	v_mfma_f32_16x16x32_bf16 v[132:135], v[60:63], v[116:119], v[132:135]
	v_mfma_f32_16x16x32_bf16 v[128:131], v[44:47], v[116:119], v[128:131]
	v_mfma_f32_16x16x32_bf16 v[136:139], v[76:79], v[116:119], v[136:139]
)ASM"
R"ASM(
	s_waitcnt lgkmcnt(0)
	v_mfma_f32_16x16x32_bf16 v[124:127], v[32:35], v[120:123], v[124:127]
	v_mfma_f32_16x16x32_bf16 v[132:135], v[64:67], v[120:123], v[132:135]
	v_mfma_f32_16x16x32_bf16 v[128:131], v[48:51], v[120:123], v[128:131]
	v_mfma_f32_16x16x32_bf16 v[136:139], v[80:83], v[120:123], v[136:139]
	s_nop 5
	v_permlane32_swap_b32 v124, v132
	v_permlane32_swap_b32 v125, v133
	v_permlane32_swap_b32 v126, v134
	v_permlane32_swap_b32 v127, v135
	v_permlane32_swap_b32 v128, v136
	v_permlane32_swap_b32 v129, v137
	v_permlane32_swap_b32 v130, v138
	v_permlane32_swap_b32 v131, v139
	v_add_f32_dpp v140, v132, v124 row_ror:8 row_mask:0xf bank_mask:0x3
	v_add_f32_dpp v141, v133, v125 row_ror:8 row_mask:0xf bank_mask:0x3
	v_add_f32_dpp v142, v134, v126 row_ror:8 row_mask:0xf bank_mask:0x3
	v_add_f32_dpp v143, v135, v127 row_ror:8 row_mask:0xf bank_mask:0x3
	v_add_f32_dpp v144, v136, v128 row_ror:8 row_mask:0xf bank_mask:0x3
	v_add_f32_dpp v145, v137, v129 row_ror:8 row_mask:0xf bank_mask:0x3
	v_add_f32_dpp v146, v138, v130 row_ror:8 row_mask:0xf bank_mask:0x3
	v_add_f32_dpp v147, v139, v131 row_ror:8 row_mask:0xf bank_mask:0x3
	v_add_f32_dpp v140, v124, v132 row_ror:8 row_mask:0xf bank_mask:0xc
	v_add_f32_dpp v141, v125, v133 row_ror:8 row_mask:0xf bank_mask:0xc
	v_add_f32_dpp v142, v126, v134 row_ror:8 row_mask:0xf bank_mask:0xc
	v_add_f32_dpp v143, v127, v135 row_ror:8 row_mask:0xf bank_mask:0xc
	v_add_f32_dpp v144, v128, v136 row_ror:8 row_mask:0xf bank_mask:0xc
	v_add_f32_dpp v145, v129, v137 row_ror:8 row_mask:0xf bank_mask:0xc
	v_add_f32_dpp v146, v130, v138 row_ror:8 row_mask:0xf bank_mask:0xc
	v_add_f32_dpp v147, v131, v139 row_ror:8 row_mask:0xf bank_mask:0xc
	; scores rounded to bf16 like the reference's product; true maximum of the wave's 32 keys; exp2; bf16 into the operand table
	v_cvt_pk_bf16_f32 v124, v140, v141
	v_cvt_pk_bf16_f32 v126, v142, v143
	v_cvt_pk_bf16_f32 v128, v144, v145
	v_cvt_pk_bf16_f32 v130, v146, v147
	v_lshlrev_b32_e32 v140, 16, v124
	v_and_b32_e32 v141, 0xffff0000, v124
	v_lshlrev_b32_e32 v142, 16, v126
	v_and_b32_e32 v143, 0xffff0000, v126
	v_lshlrev_b32_e32 v144, 16, v128
	v_and_b32_e32 v145, 0xffff0000, v128
	v_lshlrev_b32_e32 v146, 16, v130
	v_and_b32_e32 v147, 0xffff0000, v130
	v_max3_f32 v2, v140, v141, v142
	v_max3_f32 v3, v143, v144, v145
	v_max3_f32 v2, v2, v146, v147
	v_max_f32_e32 v2, v2, v3
	v_mov_b32_e32 v3, v2
	s_nop 1
	v_permlane16_swap_b32 v2, v3
	v_max_f32_e32 v2, v2, v3
	v_mov_b32_e32 v3, v2
	s_nop 1
	v_permlane32_swap_b32 v2, v3
	v_max_f32_e32 v2, v2, v3
	s_mov_b32 s47, 0x3db8aa3b
	v_mul_f32_e32 v2, s47, v2
	v_fma_f32 v140, v140, s47, -v2
	v_fma_f32 v141, v141, s47, -v2
	v_fma_f32 v142, v142, s47, -v2
	v_fma_f32 v143, v143, s47, -v2
	v_fma_f32 v144, v144, s47, -v2
	v_fma_f32 v145, v145, s47, -v2
	v_fma_f32 v146, v146, s47, -v2
	v_fma_f32 v147, v147, s47, -v2
	ds_write_b32 v14, v2 offset:64
	v_exp_f32_e32 v140, v140
	v_exp_f32_e32 v141, v141
	v_exp_f32_e32 v142, v142
	v_exp_f32_e32 v143, v143
	v_exp_f32_e32 v144, v144
	v_exp_f32_e32 v145, v145
	v_exp_f32_e32 v146, v146
	v_exp_f32_e32 v147, v147
	v_cvt_pk_bf16_f32 v124, v140, v144
	v_cvt_pk_bf16_f32 v125, v141, v145
	v_cvt_pk_bf16_f32 v126, v142, v146
	v_cvt_pk_bf16_f32 v127, v143, v147
	ds_write_b16 v13, v124 offset:8192
	ds_write_b16_d16_hi v13, v124 offset:9216
	ds_write_b16 v13, v125 offset:8320
	ds_write_b16_d16_hi v13, v125 offset:9344
	ds_write_b16 v13, v126 offset:8448
	ds_write_b16_d16_hi v13, v126 offset:9472
	ds_write_b16 v13, v127 offset:8576
	ds_write_b16_d16_hi v13, v127 offset:9600
	s_waitcnt lgkmcnt(0)
	s_mov_b64 exec, 1
	ds_add_u32 v9, v10 offset:4
	s_mov_b64 exec, -1
	v_pk_add_f32 v[140:141], v[140:141], v[142:143]
	v_pk_add_f32 v[144:145], v[144:145], v[146:147]
	v_pk_add_f32 v[140:141], v[140:141], v[144:145]
	v_add_f32_e32 v2, v140, v141
	v_mov_b32_e32 v3, v2
	s_nop 1
	v_permlane16_swap_b32 v2, v3
	v_add_f32_e32 v2, v2, v3
	v_mov_b32_e32 v3, v2
	s_nop 1
	v_permlane32_swap_b32 v2, v3
	v_add_f32_e32 v2, v2, v3
	ds_write_b32 v14, v2 offset:68
	s_waitcnt lgkmcnt(0)
	s_mov_b64 exec, 1
	ds_add_u32 v9, v10 offset:36
	s_mov_b64 exec, -1
	s_cmp_lg_u32 s5, 0
	s_cbranch_scc1 .Lfd_noml
	; ---- wave 4: publish maximum and sum of this split for both heads
	s_mov_b32 s47, 0x400000
.Lfd_w15:
	ds_read_b32 v3, v9 offset:36
	s_waitcnt lgkmcnt(0)
	v_cmp_gt_u32_e32 vcc, 4, v3
	s_cbranch_vccz .Lfd_w16
	s_sleep 1
	s_sub_u32 s47, s47, 1
	s_cbranch_scc0 .Lfd_w15
.Lfd_w16:
	ds_read_b32 v3, v9 offset:44
	s_waitcnt lgkmcnt(0)
	v_readfirstlane_b32 s34, v3
	s_cmp_ge_u32 s34, 264
	s_cselect_b32 s34, 0, 0x2000
	v_lshlrev_b32_e32 v3, 5, v15
	ds_read_b128 v[20:23], v3 offset:64
	ds_read_b128 v[24:27], v3 offset:80
	s_lshl_b32 s47, s3, 5
	s_add_u32 s47, s47, s4
	s_lshl_b32 s47, s47, 7
	s_and_b32 s52, s47, 0xffffe000
	s_add_u32 s47, s47, s52
	s_add_u32 s47, s47, s34
	s_add_u32 s47, s47, 0x30000
	v_lshl_add_u32 v2, v15, 4, s47
	v_mov_b32_e32 v29, s44
	v_mov_b32_e32 v31, s44
	s_waitcnt lgkmcnt(0)
	v_max_f32_e32 v28, v20, v22
	v_max3_f32 v28, v24, v26, v28
	v_sub_f32_e32 v20, v20, v28
	v_sub_f32_e32 v22, v22, v28
	v_sub_f32_e32 v24, v24, v28
	v_sub_f32_e32 v26, v26, v28
	v_exp_f32_e32 v20, v20
	v_exp_f32_e32 v22, v22
	v_exp_f32_e32 v24, v24
	v_exp_f32_e32 v26, v26
	v_mul_f32_e32 v30, v20, v21
	v_mul_f32_e32 v3, v24, v25
	v_fmac_f32_e32 v30, v22, v23
	v_fmac_f32_e32 v3, v26, v27
	v_add_f32_e32 v30, v30, v3
	s_mov_b64 exec, 17
	buffer_store_dwordx4 v[28:31], v2, s[28:31], 0 offen sc1
	s_mov_b64 exec, -1
	s_nop 1
.Lfd_noml:
	s_cmp_lt_u32 s4, 28
	s_cbranch_scc0 .Lfd_final
.Lfd_noffn:
	; ======== s < 28: combine the 32 splits for 32 rows per wave, times the wo tile (128 rows x 64 columns per workgroup)
	v_and_b32_e32 v3, 7, v1
	v_lshlrev_b32_e32 v16, 11, v3
	v_lshl_or_b32 v16, v18, 4, v16
	v_lshlrev_b32_e32 v17, 4, v3
	ds_read_b32 v3, v9 offset:44
	s_waitcnt lgkmcnt(0)
	v_readfirstlane_b32 s34, v3
	s_cmp_ge_u32 s34, 264
	s_cselect_b32 s34, 0, 0x2000
	v_and_b32_e32 v2, 0x2000, v16
	v_add_u32_e32 v16, v2, v16
	s_lshl_b32 s54, s3, 17
	s_lshl_b32 s47, s24, 9
	s_add_u32 s54, s54, s47
	s_lshl_b32 s47, s5, 7
	s_add_u32 s54, s54, s47
	s_add_u32 s54, s54, s34
	s_add_u32 s54, s54, 0x40000
	s_add_u32 s55, s54, 0x8000
	s_add_u32 s56, s54, 0x10000
	s_add_u32 s57, s54, 0x18000
	s_lshl_b32 s52, s3, 12
	s_and_b32 s47, s52, 0xffffe000
	s_add_u32 s52, s52, s47
	s_add_u32 s52, s52, s34
	s_lshr_b32 s47, s24, 1
	s_lshl_b32 s47, s47, 4
	s_add_u32 s52, s52, s47
	s_add_u32 s52, s52, 0x30000
	v_and_b32_e32 v19, 15, v1
	v_lshlrev_b32_e32 v19, 7, v19
	v_bfe_u32 v2, v1, 3, 1
	v_lshl_or_b32 v12, v2, 2, v17
	s_lshl_b32 s47, s5, 8
	v_add_u32_e32 v13, s47, v11
	v_and_b32_e32 v14, 31, v1
	s_lshl_b32 s47, s25, 5
	s_lshl2_add_u32 s53, s3, s24
	s_add_u32 s47, s47, s53
	s_lshl_b32 s47, s47, 9
	s_and_b32 s53, s47, 0xffffe000
	s_add_u32 s47, s47, s53
	s_add_u32 s47, s47, 0x140000
	v_lshl_add_u32 v15, v14, 4, s47
	v_add_u32_e32 v80, 0x2000, v15
	v_lshlrev_b32_e32 v14, 3, v14
	v_mov_b32_e32 v69, s44
	v_mov_b32_e32 v71, s44
	s_mov_b32 s62, 0x00ff00ff
	s_mov_b32 s63, s62
	s_mov_b32 s47, 0x400000
.Lfd_w17:
	ds_read_b32 v3, v9 offset:8
	s_waitcnt lgkmcnt(0)
	v_cmp_gt_u32_e32 vcc, 4, v3
	s_cbranch_vccz .Lfd_w18
	s_sleep 1
	s_sub_u32 s47, s47, 1
	s_cbranch_scc0 .Lfd_w17
.Lfd_w18:
	s_cmp_lg_u32 s5, 3
	s_cbranch_scc1 .Lfd_nom
	buffer_load_dwordx4 v[36:39], v19, s[28:31], s52 offen sc1
	buffer_load_dwordx4 v[40:43], v19, s[28:31], s52 offen offset:2048 sc1
	s_sleep 3
	buffer_load_dwordx4 v[20:23], v16, s[28:31], s54 offen sc1
	buffer_load_dwordx4 v[24:27], v16, s[28:31], s55 offen sc1
	buffer_load_dwordx4 v[28:31], v16, s[28:31], s56 offen sc1
	buffer_load_dwordx4 v[32:35], v16, s[28:31], s57 offen sc1
	; wave 7: global maximum and sum of the head, one weight per split
	s_mov_b32 s47, 0x10000
	s_waitcnt vmcnt(4)
.Lfd_g19:
	v_bitop3_b32 v2, v37, v39, s44 bitop3:0x7e
	v_bitop3_b32 v148, v41, v43, s44 bitop3:0x7e
	v_or_b32_e32 v2, v2, v148
	v_cmp_ne_u32_e32 vcc, 0, v2
	s_cbranch_vccz .Lfd_g20
	s_mov_b64 exec, vcc
	buffer_load_dwordx4 v[36:39], v19, s[28:31], s52 offen sc1
	buffer_load_dwordx4 v[40:43], v19, s[28:31], s52 offen offset:2048 sc1
	s_mov_b64 exec, -1
	s_sub_u32 s47, s47, 1
	s_waitcnt vmcnt(0)
	s_cbranch_scc0 .Lfd_g19
.Lfd_g20:
	v_max_f32_e32 v2, v36, v40
	s_nop 1
	v_max_f32_dpp v2, v2, v2 quad_perm:[1,0,3,2] row_mask:0xf bank_mask:0xf
	s_nop 1
	v_max_f32_dpp v2, v2, v2 quad_perm:[2,3,0,1] row_mask:0xf bank_mask:0xf
	s_nop 1
	v_max_f32_dpp v2, v2, v2 row_half_mirror row_mask:0xf bank_mask:0xf
	s_nop 1
	v_max_f32_dpp v2, v2, v2 row_ror:8 row_mask:0xf bank_mask:0xf
	v_sub_f32_e32 v36, v36, v2
	v_sub_f32_e32 v40, v40, v2
	v_exp_f32_e32 v36, v36
	v_exp_f32_e32 v40, v40
	s_nop 0
	v_mul_f32_e32 v2, v36, v38
	v_fmac_f32_e32 v2, v40, v42
	s_nop 1
	v_add_f32_dpp v2, v2, v2 quad_perm:[1,0,3,2] row_mask:0xf bank_mask:0xf
	s_nop 1
	v_add_f32_dpp v2, v2, v2 quad_perm:[2,3,0,1] row_mask:0xf bank_mask:0xf
	s_nop 1
	v_add_f32_dpp v2, v2, v2 row_half_mirror row_mask:0xf bank_mask:0xf
	s_nop 1
	v_add_f32_dpp v2, v2, v2 row_ror:8 row_mask:0xf bank_mask:0xf
	v_rcp_f32_e32 v2, v2
	s_nop 0
	v_mul_f32_e32 v36, v36, v2
	v_mul_f32_e32 v40, v40, v2
	ds_write_b32 v12, v36 offset:6656
	ds_write_b32 v12, v40 offset:6664
	s_waitcnt lgkmcnt(0)
	s_mov_b64 exec, 1
	ds_add_u32 v9, v10 offset:16
	s_mov_b64 exec, -1
	s_branch .Lfd_om
.Lfd_nom:
	s_sleep 3
	buffer_load_dwordx4 v[20:23], v16, s[28:31], s54 offen sc1
	buffer_load_dwordx4 v[24:27], v16, s[28:31], s55 offen sc1
	buffer_load_dwordx4 v[28:31], v16, s[28:31], s56 offen sc1
	buffer_load_dwordx4 v[32:35], v16, s[28:31], s57 offen sc1
.Lfd_om:
	s_mov_b32 s47, 0x400000
.Lfd_w21:
	ds_read_b32 v3, v9 offset:16
	s_waitcnt lgkmcnt(0)
	v_cmp_gt_u32_e32 vcc, 1, v3
	s_cbranch_vccz .Lfd_w22
	s_sub_u32 s47, s47, 1
	s_cbranch_scc0 .Lfd_w21
.Lfd_w22:
	ds_read_b128 v[44:47], v17 offset:6656
	; o = sum of weight x partial over the splits, rounded to bf16; GEMV; sum over the four waves; a copy into each half
	s_mov_b32 s47, 0x10000
	s_waitcnt vmcnt(0)
.Lfd_g23:
	v_bitop3_b32 v2, v21, v23, s44 bitop3:0x7e
	v_bitop3_b32 v148, v25, v27, s44 bitop3:0x7e
	v_bitop3_b32 v149, v29, v31, s44 bitop3:0x7e
	v_bitop3_b32 v150, v33, v35, s44 bitop3:0x7e
	v_or3_b32 v2, v2, v148, v149
	v_or_b32_e32 v2, v2, v150
	v_cmp_ne_u32_e32 vcc, 0, v2
	s_cbranch_vccz .Lfd_g24
	s_mov_b64 exec, vcc
	buffer_load_dwordx4 v[20:23], v16, s[28:31], s54 offen sc1
	buffer_load_dwordx4 v[24:27], v16, s[28:31], s55 offen sc1
	buffer_load_dwordx4 v[28:31], v16, s[28:31], s56 offen sc1
	buffer_load_dwordx4 v[32:35], v16, s[28:31], s57 offen sc1
	s_mov_b64 exec, -1
	s_sub_u32 s47, s47, 1
	s_waitcnt vmcnt(0)
	s_cbranch_scc0 .Lfd_g23
.Lfd_g24:
	s_waitcnt lgkmcnt(0)
	v_lshlrev_b32_e32 v48, 16, v20
	v_and_b32_e32 v49, 0xffff0000, v20
	v_lshlrev_b32_e32 v50, 16, v22
	v_and_b32_e32 v51, 0xffff0000, v22
	v_pk_mul_f32 v[56:57], v[44:45], v[48:49] op_sel:[0,0] op_sel_hi:[0,1]
	v_pk_mul_f32 v[58:59], v[44:45], v[50:51] op_sel:[0,0] op_sel_hi:[0,1]
	v_lshlrev_b32_e32 v52, 16, v24
	v_and_b32_e32 v53, 0xffff0000, v24
	v_lshlrev_b32_e32 v54, 16, v26
	v_and_b32_e32 v55, 0xffff0000, v26
	v_pk_fma_f32 v[56:57], v[44:45], v[52:53], v[56:57] op_sel:[1,0,0] op_sel_hi:[1,1,1]
	v_pk_fma_f32 v[58:59], v[44:45], v[54:55], v[58:59] op_sel:[1,0,0] op_sel_hi:[1,1,1]
	v_lshlrev_b32_e32 v48, 16, v28
	v_and_b32_e32 v49, 0xffff0000, v28
	v_lshlrev_b32_e32 v50, 16, v30
	v_and_b32_e32 v51, 0xffff0000, v30
	v_pk_fma_f32 v[56:57], v[46:47], v[48:49], v[56:57] op_sel:[0,0,0] op_sel_hi:[0,1,1]
	v_pk_fma_f32 v[58:59], v[46:47], v[50:51], v[58:59] op_sel:[0,0,0] op_sel_hi:[0,1,1]
	v_lshlrev_b32_e32 v52, 16, v32
	v_and_b32_e32 v53, 0xffff0000, v32
	v_lshlrev_b32_e32 v54, 16, v34
	v_and_b32_e32 v55, 0xffff0000, v34
	v_pk_fma_f32 v[56:57], v[46:47], v[52:53], v[56:57] op_sel:[1,0,0] op_sel_hi:[1,1,1]
	v_pk_fma_f32 v[58:59], v[46:47], v[54:55], v[58:59] op_sel:[1,0,0] op_sel_hi:[1,1,1]
	s_nop 0
	v_add_f32_dpp v56, v56, v56 quad_perm:[1,0,3,2] row_mask:0xf bank_mask:0xf
	v_add_f32_dpp v57, v57, v57 quad_perm:[1,0,3,2] row_mask:0xf bank_mask:0xf
)ASM"
R"ASM(
	v_add_f32_dpp v58, v58, v58 quad_perm:[1,0,3,2] row_mask:0xf bank_mask:0xf
	v_add_f32_dpp v59, v59, v59 quad_perm:[1,0,3,2] row_mask:0xf bank_mask:0xf
	v_add_f32_dpp v56, v56, v56 quad_perm:[2,3,0,1] row_mask:0xf bank_mask:0xf
	v_add_f32_dpp v57, v57, v57 quad_perm:[2,3,0,1] row_mask:0xf bank_mask:0xf
	v_add_f32_dpp v58, v58, v58 quad_perm:[2,3,0,1] row_mask:0xf bank_mask:0xf
	v_add_f32_dpp v59, v59, v59 quad_perm:[2,3,0,1] row_mask:0xf bank_mask:0xf
	v_add_f32_dpp v56, v56, v56 row_half_mirror row_mask:0xf bank_mask:0xf
	v_add_f32_dpp v57, v57, v57 row_half_mirror row_mask:0xf bank_mask:0xf
	v_add_f32_dpp v58, v58, v58 row_half_mirror row_mask:0xf bank_mask:0xf
	v_add_f32_dpp v59, v59, v59 row_half_mirror row_mask:0xf bank_mask:0xf
	v_cvt_pk_bf16_f32 v56, v56, v56
	v_cvt_pk_bf16_f32 v57, v57, v57
	v_cvt_pk_bf16_f32 v58, v58, v58
	v_cvt_pk_bf16_f32 v59, v59, v59
	v_and_b32_e32 v20, v4, v56
	v_and_b32_e32 v21, v5, v56
	v_and_b32_e32 v22, v6, v56
	v_and_b32_e32 v23, v7, v56
	v_and_b32_e32 v24, v4, v57
	v_and_b32_e32 v25, v5, v57
	v_and_b32_e32 v26, v6, v57
	v_and_b32_e32 v27, v7, v57
	v_mfma_f32_16x16x32_bf16 v[60:63], v[20:23], v[92:95], 0
	v_and_b32_e32 v28, v4, v58
	v_and_b32_e32 v29, v5, v58
	v_and_b32_e32 v30, v6, v58
	v_and_b32_e32 v31, v7, v58
	v_mfma_f32_16x16x32_bf16 v[60:63], v[24:27], v[96:99], v[60:63]
	v_and_b32_e32 v32, v4, v59
	v_and_b32_e32 v33, v5, v59
	v_and_b32_e32 v34, v6, v59
	v_and_b32_e32 v35, v7, v59
	v_mfma_f32_16x16x32_bf16 v[60:63], v[28:31], v[100:103], v[60:63]
	s_nop 0
	v_mfma_f32_16x16x32_bf16 v[60:63], v[32:35], v[104:107], v[60:63]
	s_nop 7
	v_permlane32_swap_b32 v60, v62
	v_permlane32_swap_b32 v61, v63
	s_nop 0
	v_add_f32_dpp v64, v62, v60 row_ror:8 row_mask:0xf bank_mask:0xf
	v_add_f32_dpp v65, v63, v61 row_ror:8 row_mask:0xf bank_mask:0xf
	s_mov_b64 exec, s[62:63]
	ds_write_b64 v13, v[64:65] offset:5632
	s_mov_b64 exec, -1
	s_waitcnt lgkmcnt(0)
	s_barrier
	s_cmp_lg_u32 s5, 2
	s_cbranch_scc1 .Lfd_cdone
	ds_read_b64 v[72:73], v14 offset:5632
	ds_read_b64 v[74:75], v14 offset:5888
	ds_read_b64 v[76:77], v14 offset:6144
	ds_read_b64 v[78:79], v14 offset:6400
	s_waitcnt lgkmcnt(2)
	v_pk_add_f32 v[72:73], v[72:73], v[74:75]
	s_waitcnt lgkmcnt(0)
	v_pk_add_f32 v[76:77], v[76:77], v[78:79]
	v_add_f32_e32 v68, v72, v76
	v_add_f32_e32 v70, v73, v77
	s_mov_b32 exec_hi, 0
	buffer_store_dwordx4 v[68:71], v15, s[28:31], 0 offen sc1
	buffer_store_dwordx4 v[68:71], v80, s[28:31], 0 offen sc1
	s_mov_b64 exec, -1
.Lfd_cdone:
	s_endpgm
.Lfd_f2:
	; ======== wave 8, second half of the FFN (h < 7)
	s_cmp_eq_u32 s3, 7
	s_cbranch_scc1 .Lfd_fwdone
	v_lshrrev_b32_e32 v18, 3, v1
	v_mov_b32_e32 v0, 0xffffffc0
	; 8 partial sums x (gate, up) x 64 hidden units, one unit per lane; rows 64 s .. of w_down, columns 64 h ..
	v_lshrrev_b32_e32 v3, 5, v1
	v_and_b32_e32 v16, 31, v1
	v_lshlrev_b32_e32 v16, 4, v16
	v_lshl_or_b32 v16, v3, 9, v16
	s_lshl_b32 s54, s4, 12
	s_add_u32 s54, s54, 0x11000
	s_mul_i32 s47, s4, 0xe000
	s_lshl_b32 s52, s3, 7
	s_add_u32 s47, s47, s52
	s_add_u32 s58, s20, s47
	s_addc_u32 s59, s21, 0
	v_and_b32_e32 v2, 7, v1
	v_lshlrev_b32_e32 v2, 4, v2
	s_movk_i32 s47, 0x380
	v_mad_u32_u24 v17, v18, s47, v2
	s_mov_b32 s47, 0x400000
.Lfd_w25:
	ds_read_b32 v3, v9 offset:12
	s_waitcnt lgkmcnt(0)
	v_cmp_gt_u32_e32 vcc, 4, v3
	s_cbranch_vccz .Lfd_w26
	s_sleep 1
	s_sub_u32 s47, s47, 1
	s_cbranch_scc0 .Lfd_w25
.Lfd_w26:
	v_lshrrev_b32_e32 v3, 5, v1
	buffer_load_dwordx4 v[20:23], v16, s[28:31], s54 offen offset:0 sc1
	buffer_load_dwordx4 v[24:27], v16, s[28:31], s54 offen offset:1024 sc1
	buffer_load_dwordx4 v[28:31], v16, s[28:31], s54 offen offset:2048 sc1
	buffer_load_dwordx4 v[32:35], v16, s[28:31], s54 offen offset:3072 sc1
	global_load_dwordx4 v[36:39], v17, s[58:59] nt
	v_add_u32_e32 v17, 0x1c00, v17
	global_load_dwordx4 v[40:43], v17, s[58:59] nt
	v_add_u32_e32 v17, 0x1c00, v17
	global_load_dwordx4 v[44:47], v17, s[58:59] nt
	v_add_u32_e32 v17, 0x1c00, v17
	global_load_dwordx4 v[48:51], v17, s[58:59] nt
	v_add_u32_e32 v17, 0x1c00, v17
	global_load_dwordx4 v[52:55], v17, s[58:59] nt
	v_add_u32_e32 v17, 0x1c00, v17
	global_load_dwordx4 v[56:59], v17, s[58:59] nt
	v_add_u32_e32 v17, 0x1c00, v17
	global_load_dwordx4 v[60:63], v17, s[58:59] nt
	v_add_u32_e32 v17, 0x1c00, v17
	global_load_dwordx4 v[64:67], v17, s[58:59] nt
	v_and_b32_e32 v2, 15, v1
	v_lshlrev_b32_e32 v2, 4, v2
	v_lshl_or_b32 v2, v3, 3, v2
	v_bfe_u32 v3, v1, 4, 1
	v_lshl_or_b32 v17, v3, 2, v2
	v_lshlrev_b32_e32 v19, 2, v18
	s_mov_b32 s47, 0x10000
	s_waitcnt vmcnt(8)
.Lfd_g27:
	v_bitop3_b32 v149, v21, v23, s46 bitop3:0x7e
	v_bitop3_b32 v148, v20, v22, s45 bitop3:0x7e
	v_lshl_or_b32 v2, v149, 1, v148
	v_bitop3_b32 v149, v25, v27, s46 bitop3:0x7e
	v_bitop3_b32 v148, v24, v26, s45 bitop3:0x7e
	v_lshl_or_b32 v148, v149, 1, v148
	v_or_b32_e32 v2, v2, v148
	v_bitop3_b32 v149, v29, v31, s46 bitop3:0x7e
	v_bitop3_b32 v148, v28, v30, s45 bitop3:0x7e
	v_lshl_or_b32 v148, v149, 1, v148
	v_or_b32_e32 v2, v2, v148
	v_bitop3_b32 v149, v33, v35, s46 bitop3:0x7e
	v_bitop3_b32 v148, v32, v34, s45 bitop3:0x7e
	v_lshl_or_b32 v148, v149, 1, v148
	v_or_b32_e32 v2, v2, v148
	v_and_b32_e32 v2, 0x3f, v2
	v_cmp_ne_u32_e32 vcc, 0, v2
	s_cbranch_vccz .Lfd_g28
	s_mov_b64 exec, vcc
	buffer_load_dwordx4 v[20:23], v16, s[28:31], s54 offen offset:0 sc1
	buffer_load_dwordx4 v[24:27], v16, s[28:31], s54 offen offset:1024 sc1
	buffer_load_dwordx4 v[28:31], v16, s[28:31], s54 offen offset:2048 sc1
	buffer_load_dwordx4 v[32:35], v16, s[28:31], s54 offen offset:3072 sc1
	s_mov_b64 exec, -1
	s_sub_u32 s47, s47, 1
	s_waitcnt vmcnt(0)
	s_cbranch_scc0 .Lfd_g27
.Lfd_g28:
	v_and_b32_e32 v148, v0, v20
	v_and_b32_e32 v149, v0, v21
	v_and_b32_e32 v150, v0, v22
	v_and_b32_e32 v151, v0, v23
	v_and_b32_e32 v24, v0, v24
	v_and_b32_e32 v25, v0, v25
	v_and_b32_e32 v26, v0, v26
	v_and_b32_e32 v27, v0, v27
	v_pk_add_f32 v[148:149], v[148:149], v[24:25]
	v_pk_add_f32 v[150:151], v[150:151], v[26:27]
	v_and_b32_e32 v28, v0, v28
	v_and_b32_e32 v29, v0, v29
	v_and_b32_e32 v30, v0, v30
	v_and_b32_e32 v31, v0, v31
	v_pk_add_f32 v[148:149], v[148:149], v[28:29]
	v_pk_add_f32 v[150:151], v[150:151], v[30:31]
	v_and_b32_e32 v32, v0, v32
	v_and_b32_e32 v33, v0, v33
	v_and_b32_e32 v34, v0, v34
	v_and_b32_e32 v35, v0, v35
	v_pk_add_f32 v[148:149], v[148:149], v[32:33]
	v_pk_add_f32 v[150:151], v[150:151], v[34:35]
	s_nop 1
	v_permlane32_swap_b32 v148, v150
	v_permlane32_swap_b32 v149, v151
	v_pk_add_f32 v[148:149], v[148:149], v[150:151]
	s_nop 1
	v_permlane16_swap_b32 v148, v149
	v_cvt_pk_bf16_f32 v148, v148, v148
	v_and_b32_e32 v148, 0xffff0000, v148
	v_cvt_pk_bf16_f32 v149, v149, v149
	v_and_b32_e32 v149, 0xffff0000, v149
	; gelu, tanh form: 0.5 g (1 + tanh u) = g / (1 + exp(-2 u))
	v_mul_f32_e32 v2, 0x3d372713, v148
	v_mul_f32_e32 v2, v2, v148
	v_fma_f32 v2, v2, v148, v148
	v_mul_f32_e32 v2, 0xc0135761, v2
	v_exp_f32_e32 v2, v2
	s_nop 0
	v_add_f32_e32 v2, 1.0, v2
	v_rcp_f32_e32 v2, v2
	s_nop 0
	v_mul_f32_e32 v2, v148, v2
	v_cvt_pk_bf16_f32 v2, v2, v2
	v_and_b32_e32 v2, 0xffff0000, v2
	v_mul_f32_e32 v2, v2, v149
	v_cvt_pk_bf16_f32 v2, v2, v2
	ds_write_b32 v17, v2 offset:6784
	ds_read_b32 v68, v19 offset:6784
	ds_read_b32 v69, v19 offset:6816
	ds_read_b32 v70, v19 offset:6848
	ds_read_b32 v71, v19 offset:6880
	ds_read_b32 v72, v19 offset:6912
	ds_read_b32 v73, v19 offset:6944
	ds_read_b32 v74, v19 offset:6976
	ds_read_b32 v75, v19 offset:7008
	s_lshl_b32 s47, s3, 5
	s_add_u32 s47, s47, s4
	s_lshl_b32 s47, s47, 9
	s_add_u32 s47, s47, 0x178000
	v_lshl_add_u32 v2, v11, 1, s47
	v_mov_b32_e32 v29, s44
	v_mov_b32_e32 v31, s44
	s_waitcnt lgkmcnt(0)
	v_and_b32_e32 v76, v4, v68
	v_and_b32_e32 v77, v5, v68
	v_and_b32_e32 v78, v6, v68
	v_and_b32_e32 v79, v7, v68
	s_waitcnt vmcnt(7)
	s_nop 0
	v_mfma_f32_16x16x32_bf16 v[20:23], v[76:79], v[36:39], 0
	v_and_b32_e32 v80, v4, v69
	v_and_b32_e32 v81, v5, v69
	v_and_b32_e32 v82, v6, v69
	v_and_b32_e32 v83, v7, v69
	s_waitcnt vmcnt(6)
	s_nop 0
	v_mfma_f32_16x16x32_bf16 v[20:23], v[80:83], v[40:43], v[20:23]
	v_and_b32_e32 v76, v4, v70
	v_and_b32_e32 v77, v5, v70
	v_and_b32_e32 v78, v6, v70
	v_and_b32_e32 v79, v7, v70
	s_waitcnt vmcnt(5)
	s_nop 0
	v_mfma_f32_16x16x32_bf16 v[20:23], v[76:79], v[44:47], v[20:23]
	v_and_b32_e32 v80, v4, v71
	v_and_b32_e32 v81, v5, v71
	v_and_b32_e32 v82, v6, v71
	v_and_b32_e32 v83, v7, v71
	s_waitcnt vmcnt(4)
	s_nop 0
	v_mfma_f32_16x16x32_bf16 v[20:23], v[80:83], v[48:51], v[20:23]
	v_and_b32_e32 v76, v4, v72
	v_and_b32_e32 v77, v5, v72
	v_and_b32_e32 v78, v6, v72
	v_and_b32_e32 v79, v7, v72
	s_waitcnt vmcnt(3)
	s_nop 0
	v_mfma_f32_16x16x32_bf16 v[20:23], v[76:79], v[52:55], v[20:23]
	v_and_b32_e32 v80, v4, v73
	v_and_b32_e32 v81, v5, v73
	v_and_b32_e32 v82, v6, v73
	v_and_b32_e32 v83, v7, v73
	s_waitcnt vmcnt(2)
	s_nop 0
	v_mfma_f32_16x16x32_bf16 v[20:23], v[80:83], v[56:59], v[20:23]
	v_and_b32_e32 v76, v4, v74
	v_and_b32_e32 v77, v5, v74
	v_and_b32_e32 v78, v6, v74
	v_and_b32_e32 v79, v7, v74
	s_waitcnt vmcnt(1)
	s_nop 0
	v_mfma_f32_16x16x32_bf16 v[20:23], v[76:79], v[60:63], v[20:23]
	v_and_b32_e32 v80, v4, v75
	v_and_b32_e32 v81, v5, v75
	v_and_b32_e32 v82, v6, v75
	v_and_b32_e32 v83, v7, v75
	s_waitcnt vmcnt(0)
	s_nop 0
	v_mfma_f32_16x16x32_bf16 v[20:23], v[80:83], v[64:67], v[20:23]
	s_nop 7
	v_permlane32_swap_b32 v20, v22
	v_permlane32_swap_b32 v21, v23
	s_nop 0
	v_add_f32_dpp v28, v22, v20 row_ror:8 row_mask:0xf bank_mask:0xf
	v_add_f32_dpp v30, v23, v21 row_ror:8 row_mask:0xf bank_mask:0xf
	s_mov_b32 s62, 0x00ff00ff
	s_mov_b32 s63, s62
	s_mov_b64 exec, s[62:63]
	buffer_store_dwordx4 v[28:31], v2, s[28:31], 0 offen sc1
	s_mov_b64 exec, -1
	s_nop 1
	s_branch .Lfd_fwdone
.Lfd_ftouch:
	; ======== wave 8 of the last 32 workgroups: first touch of the lines polled at the end (speed only)
	s_mov_b32 s47, 0x400000
.Lfd_w29:
	ds_read_b32 v3, v9 offset:0
	s_waitcnt lgkmcnt(0)
	v_cmp_gt_u32_e32 vcc, 6, v3
	s_cbranch_vccz .Lfd_w30
	s_sleep 2
	s_sub_u32 s47, s47, 1
	s_cbranch_scc0 .Lfd_w29
.Lfd_w30:
	s_cmp_lt_u32 s4, 28
	s_cbranch_scc1 .Lfd_notouch
	s_cmp_lg_u32 s6, 8
	s_cbranch_scc1 .Lfd_notouch
	ds_read_b32 v3, v9 offset:44
	s_waitcnt lgkmcnt(0)
	v_readfirstlane_b32 s34, v3
	s_cmp_ge_u32 s34, 264
	s_cselect_b32 s34, 0, 0x2000
	s_sub_u32 s47, s4, 28
	s_lshl_b32 s54, s47, 15
	s_lshl_b32 s55, s3, 17
	s_add_u32 s54, s54, s55
	s_add_u32 s54, s54, s34
	s_add_u32 s54, s54, 0x40000
	v_lshlrev_b32_e32 v3, 7, v1
	buffer_load_dword v12, v3, s[28:31], s54 offen sc1
	s_add_u32 s54, s54, 0x4000
	buffer_load_dword v13, v3, s[28:31], s54 offen sc1
	s_lshl3_add_u32 s47, s47, s3
	s_min_u32 s47, s47, 27
	s_lshr_b32 s54, s47, 2
	s_lshl_b32 s54, s54, 15
	s_and_b32 s47, s47, 3
	s_lshl_b32 s47, s47, 7
	s_add_u32 s54, s54, s47
	s_add_u32 s54, s54, 0x140000
	v_and_b32_e32 v3, 31, v1
	v_lshlrev_b32_e32 v3, 9, v3
	s_add_u32 s54, s54, s34
	v_and_b32_e32 v2, 0x2000, v3
	v_add_u32_e32 v3, v2, v3
	buffer_load_dword v14, v3, s[28:31], s54 offen sc1
.Lfd_notouch:
	; one wave: once a piece of every workgroup's q tile carries this call's tag, all have read the epoch word, and it can move on
	s_cmp_lg_u32 s4, 28
	s_cbranch_scc1 .Lfd_fwdone
	s_cmp_lg_u32 s3, 0
	s_cbranch_scc1 .Lfd_fwdone
	s_mov_b32 s47, 0x400000
.Lfd_w31:
	ds_read_b32 v3, v9 offset:8
	s_waitcnt lgkmcnt(0)
	v_cmp_gt_u32_e32 vcc, 4, v3
	s_cbranch_vccz .Lfd_w32
	s_sleep 4
	s_sub_u32 s47, s47, 1
	s_cbranch_scc0 .Lfd_w31
.Lfd_w32:
	ds_read_b32 v3, v9 offset:20
	s_waitcnt lgkmcnt(0)
	v_readfirstlane_b32 s44, v3
	s_and_b32 s45, s44, 0x3f
	s_bfe_u32 s46, s44, 0x50006
	v_and_b32_e32 v16, 1, v1
	v_lshlrev_b32_e32 v16, 12, v16
	v_and_b32_e32 v3, 2, v1
	v_lshl_or_b32 v16, v3, 10, v16
	v_and_b32_e32 v3, 28, v1
	v_lshl_or_b32 v16, v3, 6, v16
	v_and_b32_e32 v3, 32, v1
	v_lshl_or_b32 v16, v3, 8, v16
	s_mov_b32 s54, 0x1000
	s_mov_b32 s55, 0x5000
	s_mov_b32 s56, 0x9000
	s_mov_b32 s57, 0xd000
	buffer_load_dwordx4 v[20:23], v16, s[28:31], s54 offen sc1
	buffer_load_dwordx4 v[24:27], v16, s[28:31], s55 offen sc1
	buffer_load_dwordx4 v[28:31], v16, s[28:31], s56 offen sc1
	buffer_load_dwordx4 v[32:35], v16, s[28:31], s57 offen sc1
)ASM"
R"ASM(
	s_mov_b32 s47, 0x10000
	s_waitcnt vmcnt(0)
.Lfd_g33:
	v_bitop3_b32 v149, v21, v23, s46 bitop3:0x7e
	v_bitop3_b32 v148, v20, v22, s45 bitop3:0x7e
	v_lshl_or_b32 v2, v149, 1, v148
	v_bitop3_b32 v149, v25, v27, s46 bitop3:0x7e
	v_bitop3_b32 v148, v24, v26, s45 bitop3:0x7e
	v_lshl_or_b32 v148, v149, 1, v148
	v_or_b32_e32 v2, v2, v148
	v_bitop3_b32 v149, v29, v31, s46 bitop3:0x7e
	v_bitop3_b32 v148, v28, v30, s45 bitop3:0x7e
	v_lshl_or_b32 v148, v149, 1, v148
	v_or_b32_e32 v2, v2, v148
	v_bitop3_b32 v149, v33, v35, s46 bitop3:0x7e
	v_bitop3_b32 v148, v32, v34, s45 bitop3:0x7e
	v_lshl_or_b32 v148, v149, 1, v148
	v_or_b32_e32 v2, v2, v148
	v_and_b32_e32 v2, 0x3f, v2
	v_cmp_ne_u32_e32 vcc, 0, v2
	s_cbranch_vccz .Lfd_g34
	s_mov_b64 exec, vcc
	buffer_load_dwordx4 v[20:23], v16, s[28:31], s54 offen sc1
	buffer_load_dwordx4 v[24:27], v16, s[28:31], s55 offen sc1
	buffer_load_dwordx4 v[28:31], v16, s[28:31], s56 offen sc1
	buffer_load_dwordx4 v[32:35], v16, s[28:31], s57 offen sc1
	s_mov_b64 exec, -1
	s_sub_u32 s47, s47, 1
	s_waitcnt vmcnt(0)
	s_cbranch_scc0 .Lfd_g33
.Lfd_g34:
	s_mov_b64 exec, 1
	v_mov_b32_e32 v3, s44
	buffer_store_dword v3, off, s[28:31], 0 sc1
	s_mov_b64 exec, -1
.Lfd_fwdone:
	s_endpgm
.Lfd_final:
	; ======== waves 6, 7 of 28 of the last 32 workgroups: 8 output columns each; out = x + s silu(f)
	s_sub_u32 s24, s4, 28
	s_lshl3_add_u32 s24, s24, s3
	s_cmp_ge_u32 s24, 28
	s_cbranch_scc1 .Lfd_fdone
	s_cmp_lt_u32 s5, 2
	s_cbranch_scc1 .Lfd_fdone
	s_lshl_b32 s25, s24, 1
	s_add_u32 s25, s25, s5
	s_sub_u32 s25, s25, 2
	s_lshr_b32 s54, s25, 3
	s_lshl_b32 s54, s54, 14
	s_and_b32 s47, s25, 7
	s_lshl_b32 s47, s47, 6
	s_add_u32 s54, s54, s47
	s_add_u32 s56, s54, 0x178000
	s_add_u32 s57, s56, 0x2000
	ds_read_b32 v3, v9 offset:44
	s_waitcnt lgkmcnt(0)
	v_readfirstlane_b32 s34, v3
	s_cmp_ge_u32 s34, 264
	s_cselect_b32 s34, 0, 0x2000
	s_and_b32 s55, s54, 0xffffc000
	s_add_u32 s54, s54, s55
	s_add_u32 s54, s54, s34
	s_add_u32 s54, s54, 0x140000
	s_add_u32 s55, s54, 0x4000
	v_and_b32_e32 v2, 15, v1
	v_lshlrev_b32_e32 v16, 9, v2
	v_and_or_b32 v16, v1, 48, v16
	v_cmp_gt_u32_e64 s[48:49], 12, v2
	v_add_u32_e32 v3, 0xfffff800, v16
	v_lshrrev_b32_e32 v19, 4, v1
	v_cndmask_b32_e64 v17, v3, v16, s[48:49]
	s_lshl_b32 s47, s25, 4
	v_lshl_add_u32 v19, v19, 2, s47
	buffer_load_dword v12, v19, s[40:43], 0 offen
	s_mov_b32 s36, s22
	s_mov_b32 s37, s23
	s_mov_b32 s38, s42
	s_mov_b32 s39, s43
	s_load_dword s33, s[22:23], 0
	s_mov_b32 s47, 0x400000
.Lfd_w35:
	ds_read_b32 v3, v9 offset:8
	s_waitcnt lgkmcnt(0)
	v_cmp_gt_u32_e32 vcc, 4, v3
	s_cbranch_vccz .Lfd_w36
	s_sleep 1
	s_sub_u32 s47, s47, 1
	s_cbranch_scc0 .Lfd_w35
.Lfd_w36:
	s_sleep 34
	buffer_load_dwordx4 v[20:23], v16, s[28:31], s56 offen sc1
	buffer_load_dwordx4 v[24:27], v17, s[28:31], s57 offen sc1
	s_mov_b32 s47, 0x10000
	s_waitcnt vmcnt(0)
.Lfd_g37:
	v_bitop3_b32 v2, v21, v23, s44 bitop3:0x7e
	v_bitop3_b32 v148, v25, v27, s44 bitop3:0x7e
	v_or_b32_e32 v2, v2, v148
	v_cmp_ne_u32_e32 vcc, 0, v2
	s_cbranch_vccz .Lfd_g38
	s_mov_b64 exec, vcc
	buffer_load_dwordx4 v[20:23], v16, s[28:31], s56 offen sc1
	buffer_load_dwordx4 v[24:27], v17, s[28:31], s57 offen sc1
	s_mov_b64 exec, -1
	s_sub_u32 s47, s47, 1
	s_waitcnt vmcnt(0)
	s_cbranch_scc0 .Lfd_g37
.Lfd_g38:
	buffer_load_dwordx4 v[28:31], v16, s[28:31], s54 offen sc1
	buffer_load_dwordx4 v[32:35], v16, s[28:31], s55 offen sc1
	v_cndmask_b32_e64 v24, 0, v24, s[48:49]
	v_cndmask_b32_e64 v26, 0, v26, s[48:49]
	v_add_f32_e32 v36, v20, v24
	v_add_f32_e32 v37, v22, v26
	s_nop 0
	v_add_f32_dpp v36, v36, v36 quad_perm:[1,0,3,2] row_mask:0xf bank_mask:0xf
	v_add_f32_dpp v37, v37, v37 quad_perm:[1,0,3,2] row_mask:0xf bank_mask:0xf
	s_nop 0
	v_add_f32_dpp v36, v36, v36 quad_perm:[2,3,0,1] row_mask:0xf bank_mask:0xf
	v_add_f32_dpp v37, v37, v37 quad_perm:[2,3,0,1] row_mask:0xf bank_mask:0xf
	s_nop 0
	v_add_f32_dpp v36, v36, v36 row_half_mirror row_mask:0xf bank_mask:0xf
	v_add_f32_dpp v37, v37, v37 row_half_mirror row_mask:0xf bank_mask:0xf
	s_nop 0
	v_add_f32_dpp v36, v36, v36 row_ror:8 row_mask:0xf bank_mask:0xf
	v_add_f32_dpp v37, v37, v37 row_ror:8 row_mask:0xf bank_mask:0xf
	v_cvt_pk_bf16_f32 v36, v36, v37
	v_and_b32_e32 v37, 0xffff0000, v36
	v_lshlrev_b32_e32 v36, 16, v36
	v_mul_f32_e32 v38, 0xbfb8aa3b, v36
	v_mul_f32_e32 v39, 0xbfb8aa3b, v37
	v_exp_f32_e32 v38, v38
	v_exp_f32_e32 v39, v39
	s_nop 0
	v_add_f32_e32 v38, 1.0, v38
	v_add_f32_e32 v39, 1.0, v39
	v_rcp_f32_e32 v38, v38
	v_rcp_f32_e32 v39, v39
	s_nop 0
	v_pk_mul_f32 v[36:37], v[36:37], v[38:39]
	v_cvt_pk_bf16_f32 v36, v36, v37
	v_and_b32_e32 v37, 0xffff0000, v36
	v_lshlrev_b32_e32 v36, 16, v36
	v_lshlrev_b32_e32 v40, 16, v12
	v_and_b32_e32 v41, 0xffff0000, v12
	s_mov_b32 s62, 0x00010001
	s_mov_b32 s63, s62
	; two passes in flight, each into registers of its own; the first complete one is used
	s_waitcnt vmcnt(0)
	s_mov_b32 s47, 0x10000
	buffer_load_dwordx4 v[28:31], v16, s[28:31], s54 offen sc1
	buffer_load_dwordx4 v[32:35], v16, s[28:31], s55 offen sc1
	s_sleep 2
	buffer_load_dwordx4 v[44:47], v16, s[28:31], s54 offen sc1
	buffer_load_dwordx4 v[48:51], v16, s[28:31], s55 offen sc1
.Lfd_pp:
	s_waitcnt vmcnt(2)
	v_bitop3_b32 v2, v29, v31, s44 bitop3:0x7e
	v_bitop3_b32 v148, v33, v35, s44 bitop3:0x7e
	v_or_b32_e32 v2, v2, v148
	v_cmp_ne_u32_e32 vcc, 0, v2
	s_cbranch_vccz .Lfd_pp0
	s_mov_b64 exec, vcc
	buffer_load_dwordx4 v[28:31], v16, s[28:31], s54 offen sc1
	buffer_load_dwordx4 v[32:35], v16, s[28:31], s55 offen sc1
	s_mov_b64 exec, -1
	s_waitcnt vmcnt(2)
	v_bitop3_b32 v2, v45, v47, s44 bitop3:0x7e
	v_bitop3_b32 v148, v49, v51, s44 bitop3:0x7e
	v_or_b32_e32 v2, v2, v148
	v_cmp_ne_u32_e32 vcc, 0, v2
	s_cbranch_vccz .Lfd_pp1
	s_mov_b64 exec, vcc
	buffer_load_dwordx4 v[44:47], v16, s[28:31], s54 offen sc1
	buffer_load_dwordx4 v[48:51], v16, s[28:31], s55 offen sc1
	s_mov_b64 exec, -1
	s_sub_u32 s47, s47, 1
	s_cbranch_scc0 .Lfd_pp
.Lfd_pp1:
	v_add_f32_e32 v38, v44, v48
	v_add_f32_e32 v39, v46, v50
	s_branch .Lfd_ppd
.Lfd_pp0:
	v_add_f32_e32 v38, v28, v32
	v_add_f32_e32 v39, v30, v34
.Lfd_ppd:
	s_nop 0
	v_add_f32_dpp v38, v38, v38 quad_perm:[1,0,3,2] row_mask:0xf bank_mask:0xf
	v_add_f32_dpp v39, v39, v39 quad_perm:[1,0,3,2] row_mask:0xf bank_mask:0xf
	s_nop 0
	v_add_f32_dpp v38, v38, v38 quad_perm:[2,3,0,1] row_mask:0xf bank_mask:0xf
	v_add_f32_dpp v39, v39, v39 quad_perm:[2,3,0,1] row_mask:0xf bank_mask:0xf
	s_nop 0
	v_add_f32_dpp v38, v38, v38 row_half_mirror row_mask:0xf bank_mask:0xf
	v_add_f32_dpp v39, v39, v39 row_half_mirror row_mask:0xf bank_mask:0xf
	s_nop 0
	v_add_f32_dpp v38, v38, v38 row_ror:8 row_mask:0xf bank_mask:0xf
	v_add_f32_dpp v39, v39, v39 row_ror:8 row_mask:0xf bank_mask:0xf
	v_cvt_pk_bf16_f32 v38, v38, v39
	v_and_b32_e32 v39, 0xffff0000, v38
	v_lshlrev_b32_e32 v38, 16, v38
	v_pk_mul_f32 v[38:39], v[38:39], v[36:37]
	v_cvt_pk_bf16_f32 v38, v38, v39
	v_and_b32_e32 v39, 0xffff0000, v38
	v_lshlrev_b32_e32 v38, 16, v38
	v_pk_add_f32 v[38:39], v[38:39], v[40:41]
	v_cvt_pk_bf16_f32 v38, v38, v39
	s_mov_b64 exec, s[62:63]
	buffer_store_dword v38, v19, s[36:39], 0 offen
.Lfd_fend:
	s_mov_b64 exec, -1
.Lfd_fdone:
	s_endpgm
.Lfd_end:
)ASM"
      :
      : "{s[0:1]}"(__builtin_amdgcn_kernarg_segment_ptr()), "{s2}"(__builtin_amdgcn_workgroup_id_x()), "{v0}"(__builtin_amdgcn_workitem_id_x())
      : "v1", "v2", "v3", "v4", "v5", "v6", "v7", "v8", "v9", "v10", "v11", "v12", "v13", "v14", "v15", "v16", "v17", "v18", "v19", "v20", "v21", "v22", "v23", "v24", "v25", "v26", "v27", "v28", "v29", "v30", "v31", "v32", "v33", "v34", "v35", "v36", "v37", "v38", "v39", "v40", "v41", "v42", "v43", "v44", "v45", "v46", "v47", "v48", "v49", "v50", "v51", "v52", "v53", "v54", "v55", "v56", "v57", "v58", "v59", "v60", "v61", "v62", "v63", "v64", "v65", "v66", "v67", "v68", "v69", "v70", "v71", "v72", "v73", "v74", "v75", "v76", "v77", "v78", "v79", "v80", "v81", "v82", "v83", "v84", "v85", "v86", "v87", "v88", "v89", "v90", "v91", "v92", "v93", "v94", "v95", "v96", "v97", "v98", "v99", "v100", "v101", "v102", "v103", "v104", "v105", "v106", "v107", "v108", "v109", "v110", "v111", "v112", "v113", "v114", "v115", "v116", "v117", "v118", "v119", "v120", "v121", "v122", "v123", "v124", "v125", "v126", "v127", "v128", "v129", "v130", "v131", "v132", "v133", "v134", "v135", "v136", "v137", "v138", "v139", "v140", "v141", "v142", "v143", "v144", "v145", "v146", "v147", "v148", "v149", "v150", "v151", "v152", "v153", "v154", "v155", "v156", "v157", "v158", "v159", "v160", "v161", "v162", "v163", "s3", "s4", "s5", "s6", "s7", "s8", "s9", "s10", "s11", "s12", "s13", "s14", "s15", "s16", "s17", "s18", "s19", "s20", "s21", "s22", "s23", "s24", "s25", "s26", "s27", "s28", "s29", "s30", "s31", "s33", "s34", "s35", "s36", "s37", "s38", "s39", "s40", "s41", "s42", "s43", "s44", "s45", "s46", "s47", "s48", "s49", "s50", "s51", "s52", "s53", "s54", "s55", "s56", "s57", "s58", "s59", "s60", "s61", "s62", "s63", "vcc", "scc", "memory");
#endif
}

extern "C" void kernel_launch(void* const* d_in, const int* in_sizes, int n_in, void* d_out, int out_size, void* d_ws, size_t ws_size,
                              hipStream_t stream) {
  constexpr int LDS_BYTES = 40 * 1024;
  hipLaunchKernelGGL(fused_decode, dim3(256), dim3(576), LDS_BYTES, stream, d_in[0], d_ws, d_in[3], d_in[1], d_in[6], d_in[7], d_in[2], d_in[4], d_in[8], d_out);
}
```
